```python
import torch

def _softmax(x, axis=-1):
    return torch.softmax(x, dim=axis)
def _gelu(x):
    return torch.nn.functional.gelu(x, approximate="tanh")
def _silu(x):
    return torch.nn.functional.silu(x)
def _sigmoid(x):
    return torch.sigmoid(x)
def _layernorm(x, axis=-1, eps=1e-5):
    m = x.mean(dim=axis, keepdim=True)
    v = x.var(dim=axis, keepdim=True, unbiased=False)
    return (x - m) / torch.sqrt(v + eps)
def _scatter_add(tgt, idx, src, axis=0):
    return tgt.clone().index_add_(axis, idx, src)
def _segment_sum(src, seg, n):
    out = torch.zeros((n,) + src.shape[1:], dtype=src.dtype)
    return out.index_add_(0, seg, src)
def _pad(x, pw):
    flat = tuple(v for pair in reversed(pw) for v in pair)
    return torch.nn.functional.pad(x, flat)

kernel_name = 'standalone__gqa_softmax__sampling'

def setup_inputs():

    _w = (lambda a: (lambda t: t.to(torch.bfloat16) if t.is_floating_point() else t)(torch.as_tensor(a)))
    B, Hq, Hkv, Sq, Sk, D = 16, 32, 8, 4, 2048, 128
    return {
        "q": _w((lambda *s: torch.randn(*s, dtype=torch.bfloat16) * 0.8)(B, Hq, Sq, D)),
        "k": _w((lambda *s: torch.randn(*s, dtype=torch.bfloat16) * 0.8)(B, Hkv, Sk, D)),
        "v": _w((lambda *s: torch.randn(*s, dtype=torch.bfloat16) * 0.05)(B, Hkv, Sk, D)),
    }

def reference(q, k, v):
    B, Hq, Sq, D = q.shape
    Hkv = k.shape[1]
    rep = Hq // Hkv
    k = (lambda x, n, axis: torch.repeat_interleave(x, n, dim=axis))(k, rep, axis=1)
    v = (lambda x, n, axis: torch.repeat_interleave(x, n, dim=axis))(v, rep, axis=1)
    scale = D ** -0.5
    scores = torch.einsum("bhqd,bhkd->bhqk", q, k) * scale
    p = _softmax(scores, axis=-1)
    return torch.einsum("bhqk,bhkd->bhqd", p, v)
```

```cpp
#include <hip/hip_runtime.h>
#include <hip/hip_bf16.h>
#include <stdint.h>
#include <type_traits>

#define NTA 32
#define CKPT 1
#define PXOR 5
#define NTSTR " nt"
#define KPOS 1
#define VPOS 1

typedef __attribute__((ext_vector_type(4))) unsigned u32x4;
typedef __attribute__((ext_vector_type(2))) unsigned u32x2;
typedef __attribute__((ext_vector_type(4))) float f32x4;
typedef __attribute__((ext_vector_type(4))) short s16x4;
typedef __attribute__((ext_vector_type(8))) short s16x8;
typedef __attribute__((ext_vector_type(8))) __bf16 bf16x8;
#define LAS __attribute__((address_space(3)))
#define GAS __attribute__((address_space(1)))

constexpr int B = 16, HQ = 32, HKV = 8, SQ = 4, SK = 2048, D = 128;
constexpr int NBH = B * HKV;
constexpr int NW = 4;
constexpr int TILE = 32;
constexpr int NTILES = SK / TILE;
constexpr float CEXP = 0.08838834764831845f * 1.4426950408889634f;
constexpr int SLOT = 8192, RING = 2 * SLOT;
constexpr int LDS_Q = NW * RING;
constexpr int LDS_SM = LDS_Q + 4096;
constexpr int LDS_SL = LDS_SM + 256;
constexpr int LDS_CW = LDS_SL + 256;
constexpr int LDS_BYTES = 96 * 1024;
constexpr int OROW = 528;
constexpr unsigned SPIN_LIMIT = 1u << 16;
constexpr int WS_STATS = 8192;
constexpr int WS_JOIN = WS_STATS + 256;
constexpr int WS_ARR = WS_JOIN + 128;
constexpr int WS_PAYLOAD = WS_ARR + 128;
constexpr int WS_C = WS_PAYLOAD;
constexpr int WS_ACK = WS_C + 128;
constexpr int WS_PAIR = WS_ACK + NW * 128;
constexpr unsigned ROLE_COMBINE = 0, ROLE_PUBLISH = 1, ROLE_PUBLISH_UNSURE = 2;
static_assert((HQ / HKV) * SQ == 16, "16 packed query rows per kv head");
static_assert(NTA >= NW && NTILES - NTA >= NW, "every wave owns at least one tile");
static_assert(LDS_CW + 48 <= LDS_BYTES && 16 * OROW <= RING, "LDS layout");

__device__ __forceinline__ unsigned cvtpk(float lo, float hi) {
  unsigned r;
  asm("v_cvt_pk_bf16_f32 %0, %1, %2" : "=v"(r) : "v"(lo), "v"(hi));
  return r;
}
__device__ __forceinline__ float xmax(float x) {
  auto s = __builtin_amdgcn_permlane16_swap(__float_as_uint(x), __float_as_uint(x), false, false);
  x = fmaxf(__uint_as_float(s[0]), __uint_as_float(s[1]));
  auto t = __builtin_amdgcn_permlane32_swap(__float_as_uint(x), __float_as_uint(x), false, false);
  return fmaxf(__uint_as_float(t[0]), __uint_as_float(t[1]));
}
__device__ __forceinline__ float xsum(float x) {
  auto s = __builtin_amdgcn_permlane16_swap(__float_as_uint(x), __float_as_uint(x), false, false);
  x = __uint_as_float(s[0]) + __uint_as_float(s[1]);
  auto t = __builtin_amdgcn_permlane32_swap(__float_as_uint(x), __float_as_uint(x), false, false);
  return __uint_as_float(t[0]) + __uint_as_float(t[1]);
}
__device__ __forceinline__ bf16x8 asbf(u32x4 v) { return __builtin_bit_cast(bf16x8, v); }

__device__ __forceinline__ void dma4(unsigned m0v, __amdgpu_buffer_rsrc_t srd, unsigned soff, unsigned v0, unsigned v1,
                                     unsigned v2, unsigned v3) {
  asm volatile(
      "s_mov_b32 m0, %0\n\t"
      "s_nop 0\n\t"
      "buffer_load_dwordx4 %3, %1, %2 offen" NTSTR " lds\n\t"
      "buffer_load_dwordx4 %4, %1, %2 offen offset:1024" NTSTR " lds\n\t"
      "buffer_load_dwordx4 %5, %1, %2 offen offset:2048" NTSTR " lds\n\t"
      "buffer_load_dwordx4 %6, %1, %2 offen offset:3072" NTSTR " lds"
      :
      : "s"(m0v), "s"(srd), "s"(soff), "v"(v0), "v"(v1), "v"(v2), "v"(v3)
      : "memory");
}
__device__ __forceinline__ void dma1(unsigned m0v, __amdgpu_buffer_rsrc_t srd, unsigned v0) {
  asm volatile(
      "s_mov_b32 m0, %0\n\t"
      "s_nop 0\n\t"
      "buffer_load_dwordx4 %2, %1, 0 offen lds"
      :
      : "s"(m0v), "s"(srd), "v"(v0)
      : "memory");
}
template <int N>
__device__ __forceinline__ void wait_vm() { asm volatile("s_waitcnt vmcnt(%0)" ::"i"(N) : "memory"); }
__device__ __forceinline__ void wait_lgkm() { asm volatile("s_waitcnt lgkmcnt(0)" ::: "memory"); }

__device__ __forceinline__ unsigned ld_agent(const unsigned* p) { return __hip_atomic_load((GAS const unsigned*)p, __ATOMIC_RELAXED, __HIP_MEMORY_SCOPE_AGENT); }
__device__ __forceinline__ void st_agent(unsigned* p, unsigned v) { __hip_atomic_store((GAS unsigned*)p, v, __ATOMIC_RELAXED, __HIP_MEMORY_SCOPE_AGENT); }
__device__ __forceinline__ u32x4 tag4(f32x4 x, unsigned t) {
  const u32x4 u = __builtin_bit_cast(u32x4, x);
  return u32x4{(u[0] & ~3u) | t, (u[1] & ~3u) | t, (u[2] & ~3u) | t, (u[3] & ~3u) | t};
}
__device__ __forceinline__ bool tagged(u32x4 a, u32x4 b, u32x4 c, unsigned t) {
  const unsigned x = ((a[0] ^ t) | (a[1] ^ t) | (a[2] ^ t) | (a[3] ^ t)) | ((b[0] ^ t) | (b[1] ^ t) | (b[2] ^ t) | (b[3] ^ t)) |
                     ((c[0] ^ t) | (c[1] ^ t) | (c[2] ^ t) | (c[3] ^ t));
  return (x & 3u) == 0;
}

__device__ __forceinline__ void combine_store(f32x4 o0, f32x4 o1, float M, float L, u32x4 ra0, u32x4 ra1, float MA, unsigned la, void* dst) {
#pragma clang fp contract(off)
  const float Mt = fmaxf(M, MA);
  const float fb = __builtin_amdgcn_exp2f((M - Mt) * CEXP), fa = __builtin_amdgcn_exp2f((MA - Mt) * CEXP);
  const float LB = __uint_as_float(__float_as_uint(L) & ~3u), LA = __uint_as_float(la & ~3u);
  const float inv = 1.0f / (LB * fb + LA * fa);
  const float sb = fb * inv, sa = fa * inv;
  const u32x4 b0 = tag4(o0, 0), b1 = tag4(o1, 0);
  float r[8];
#pragma unroll
  for (int e = 0; e < 4; ++e) {
    r[e] = __uint_as_float(b0[e]) * sb + __uint_as_float(ra0[e] & ~3u) * sa;
    r[4 + e] = __uint_as_float(b1[e]) * sb + __uint_as_float(ra1[e] & ~3u) * sa;
  }
  const u32x4 o = {cvtpk(r[0], r[1]), cvtpk(r[2], r[3]), cvtpk(r[4], r[5]), cvtpk(r[6], r[7])};
  *(u32x4*)dst = o;
}

constexpr int NTHREADS = (NW + 1) * 64;

#define STAMP(i) do { } while (0)

__global__ __launch_bounds__(NTHREADS) void attn_fwd(const void* __restrict__ q, const void* __restrict__ k, const void* __restrict__ v,
                                                     void* __restrict__ out, char* __restrict__ ws
) {
  extern __shared__ __attribute__((aligned(16))) char smem[];
  asm volatile("" ::"s"(q), "s"(k), "s"(v), "s"(out), "s"(ws));
  const int tid = threadIdx.x, lane = tid & 63;
  const int w = __builtin_amdgcn_readfirstlane(tid >> 6);
  const int x8 = blockIdx.x & 7;
  const bool isP = x8 < 4;
  const int bh = (blockIdx.x >> 3) * 4 + (isP ? x8 : (x8 ^ 5));
  const int i = lane & 15, g = lane >> 4;
  const unsigned lds0 = (unsigned)(size_t)(LAS char*)smem;
  char* const pair = ws + (size_t)bh * WS_PAIR;
  const auto prs = __builtin_amdgcn_make_buffer_rsrc((void*)pair, 0, WS_PAYLOAD, 0x00020000);
  unsigned* const pC = (unsigned*)(pair + WS_C);
  STAMP(0);

  if (w == NW) {
    LAS unsigned* const cw = (LAS unsigned*)((LAS char*)smem + LDS_CW);
    unsigned* const pJoin = (unsigned*)(pair + WS_JOIN);
    const unsigned c0 = ld_agent(pC);
    const unsigned a0 = ld_agent((const unsigned*)(pair + WS_ACK + (lane & 3) * 128));
    if (lane < 12) cw[lane] = 0u;
    wait_lgkm();
    __builtin_amdgcn_s_barrier();
    unsigned c = c0;
    bool dyn = false;
    if (!isP) {
      const u32x4 z = {0u, 0u, 0u, 0u};
#pragma unroll
      for (int j = 0; j < 9; ++j) __builtin_amdgcn_raw_buffer_store_b128(z, prs, lane * 16 + j * 1024, 0, 16);
      c = (c0 & 3u) == 1u ? 2u : 1u;
      wait_vm<0>();
      if (lane == 0) { st_agent(pC, c); cw[0] = c; }
    }
    if (lane < 4) cw[4 + lane] = a0;
    STAMP(1);
    for (unsigned spin = 0; spin < SPIN_LIMIT; ++spin) {
      if (!dyn) {
        if (isP) {
          const unsigned x = ld_agent(pC);
          if (x != c0) {
            dyn = true;
            c = x;
            if (lane == 0) { st_agent(pJoin, c); cw[0] = c; }
          }
        } else {
          dyn = ld_agent(pJoin) == c;
        }
      }
      const u32x4 f = *(volatile LAS u32x4*)(cw + 8);
      if ((f[0] & f[1] & f[2] & f[3]) != 0) break;
      __builtin_amdgcn_s_sleep(4);
    }
    STAMP(3);
    unsigned role = isP ? ROLE_PUBLISH_UNSURE : ROLE_COMBINE;
    if (dyn) {
      unsigned old = 0;
      if (lane == 0) old = __hip_atomic_fetch_add((GAS unsigned*)(pair + WS_ARR), 1u, __ATOMIC_RELAXED, __HIP_MEMORY_SCOPE_AGENT);
      role = __builtin_amdgcn_readfirstlane(old) == 0 ? ROLE_PUBLISH : ROLE_COMBINE;
    }
    if (lane == 0) { cw[1] = c0; cw[2] = role; }
    wait_lgkm();
    STAMP(4);
    return;
  }

  const int nt = isP ? NTA : NTILES - NTA;
  const size_t kvoff = ((size_t)bh * SK + (isP ? 0 : NTA * TILE)) * (D * 2);
  const auto krs = __builtin_amdgcn_make_buffer_rsrc((void*)((const char*)k + kvoff), 0, nt * TILE * D * 2, 0x00020000);
  const auto vrs = __builtin_amdgcn_make_buffer_rsrc((void*)((const char*)v + kvoff), 0, nt * TILE * D * 2, 0x00020000);
  const auto qrs = __builtin_amdgcn_make_buffer_rsrc((void*)((const char*)q + (size_t)bh * 16 * D * 2), 0, 16 * D * 2, 0x00020000);

  unsigned kv[4], vv[2];
#pragma unroll
  for (int j = 0; j < 4; ++j) kv[j] = g * 256 + ((i ^ (4 * j + g)) * 16);
#pragma unroll
  for (int j = 0; j < 2; ++j) vv[j] = g * 256 + ((i ^ (8 * j + 2 * g)) * 16);

  const unsigned ring = lds0 + w * RING;
  unsigned so = w * (TILE * 256);
  auto issueK = [&](unsigned o) { dma4(ring, krs, o, kv[0], kv[1], kv[2], kv[3]); dma4(ring + 4096, krs, o + 4096, kv[0], kv[1], kv[2], kv[3]); };
  auto issueV = [&](unsigned o) { dma4(ring + SLOT, vrs, o, vv[0], vv[1], vv[0], vv[1]); dma4(ring + SLOT + 4096, vrs, o + 4096, vv[0], vv[1], vv[0], vv[1]); };

  dma1(lds0 + LDS_Q + w * 1024, qrs, w * 1024 + g * 256 + ((i ^ (4 * w + g)) * 16));
  issueK(so);
  issueV(so);

  LAS char* const ringp = (LAS char*)smem + w * RING;
  unsigned ka[4];
#pragma unroll
  for (int s = 0; s < 4; ++s) ka[s] = i * 256 + (((4 * s) ^ g ^ i) * 16);
  const int q_ = i >> 2, p_ = i & 3;
  const unsigned va0 = (4 * g + q_) * 256 + (((8 * (g & 1) + 2 * q_) + (p_ >> 1)) * 16) + (p_ & 1) * 8;

  wait_vm<16>();
  __builtin_amdgcn_s_barrier();
  asm volatile("" ::: "memory");
  u32x4 Qf[4];
#pragma unroll
  for (int s = 0; s < 4; ++s) Qf[s] = *(LAS u32x4*)((LAS char*)smem + LDS_Q + ka[s]);

  f32x4 acc[8];
#pragma unroll
  for (int e = 0; e < 8; ++e) acc[e] = f32x4{0.f, 0.f, 0.f, 0.f};
  float m = -__builtin_inff(), l = 0.f;

  const int qq = tid >> 4, jj = tid & 15;
  u32x4 ra0, ra1, rst;
  unsigned tspec = 0;
  auto fetchA = [&]() {
    ra0 = __builtin_amdgcn_raw_buffer_load_b128(prs, tid * 16, 0, 16);
    ra1 = __builtin_amdgcn_raw_buffer_load_b128(prs, 4096 + tid * 16, 0, 16);
    rst = __builtin_amdgcn_raw_buffer_load_b128(prs, WS_STATS + qq * 16, 0, 16);
  };

  auto tile = [&](auto lastc) {
    constexpr bool LAST = decltype(lastc)::value;
    f32x4 s0 = {0.f, 0.f, 0.f, 0.f}, s1 = {0.f, 0.f, 0.f, 0.f};
    u32x4 kf[8];
#pragma unroll
    for (int s = 0; s < 4; ++s) {
      kf[2 * s] = *(LAS u32x4*)(ringp + ka[s]);
      kf[2 * s + 1] = *(LAS u32x4*)(ringp + 4096 + ka[s]);
    }
    wait_lgkm();
    if (!LAST && KPOS == 0) issueK(so);
    __builtin_amdgcn_sched_barrier(0);
#pragma unroll
    for (int s = 0; s < 4; ++s) {
      s0 = __builtin_amdgcn_mfma_f32_16x16x32_bf16(asbf(kf[2 * s]), asbf(Qf[s]), s0, 0, 0, 0);
      s1 = __builtin_amdgcn_mfma_f32_16x16x32_bf16(asbf(kf[2 * s + 1]), asbf(Qf[s]), s1, 0, 0, 0);
    }
    float tm = fmaxf(fmaxf(fmaxf(s0[0], s0[1]), fmaxf(s0[2], s0[3])), fmaxf(fmaxf(s1[0], s1[1]), fmaxf(s1[2], s1[3])));
    tm = xmax(tm);
    if (!LAST && KPOS == 1) { __builtin_amdgcn_sched_barrier(0); issueK(so); __builtin_amdgcn_sched_barrier(0); }
    const float mn = fmaxf(m, tm);
    if (__builtin_amdgcn_ballot_w64(mn > m)) {
      const float alpha = __builtin_amdgcn_exp2f((m - mn) * CEXP);
      l *= alpha;
#pragma unroll
      for (int e = 0; e < 8; ++e) acc[e] *= alpha;
      m = mn;
    }
    const float mc = -m * CEXP;
    float p[8];
#pragma unroll
    for (int r = 0; r < 4; ++r) {
      p[r] = __builtin_amdgcn_exp2f(fmaf(s0[r], CEXP, mc));
      p[4 + r] = __builtin_amdgcn_exp2f(fmaf(s1[r], CEXP, mc));
    }
    l += ((p[0] + p[1]) + (p[2] + p[3])) + ((p[4] + p[5]) + (p[6] + p[7]));
    const u32x4 P = {cvtpk(p[0], p[1]), cvtpk(p[2], p[3]), cvtpk(p[4], p[5]), cvtpk(p[6], p[7])};
    __builtin_amdgcn_sched_barrier(0);
    if (!LAST && KPOS == 2) issueK(so);
    if (LAST) tspec = *(volatile LAS unsigned*)((LAS char*)smem + LDS_CW);
    if (!LAST) wait_vm<8>();
    else { wait_vm<0>(); fetchA(); }
    s16x4 vf[16];
#pragma unroll
    for (int cb = 0; cb < 8; ++cb) {
      const unsigned a = va0 ^ (cb << 5);
      vf[2 * cb] = __builtin_amdgcn_ds_read_tr16_b64_v4i16((LAS s16x4*)(ringp + SLOT + a));
      vf[2 * cb + 1] = __builtin_amdgcn_ds_read_tr16_b64_v4i16((LAS s16x4*)(ringp + SLOT + 4096 + a));
    }
    wait_lgkm();
    if (!LAST && VPOS == 0) issueV(so);
    __builtin_amdgcn_sched_barrier(0);
#pragma unroll
    for (int cb = 0; cb < 8; ++cb) {
      const s16x8 f = __builtin_shufflevector(vf[2 * cb], vf[2 * cb + 1], 0, 1, 2, 3, 4, 5, 6, 7);
      acc[cb] = __builtin_amdgcn_mfma_f32_16x16x32_bf16(__builtin_bit_cast(bf16x8, f), asbf(P), acc[cb], 0, 0, 0);
    }
    if (!LAST && VPOS == 1) { __builtin_amdgcn_sched_barrier(0); issueV(so); }
  };

  wait_vm<8>();
  STAMP(1);
#pragma unroll 1
  for (int left = (nt - w + NW - 1) / NW - 1; left > 0; --left) {
    so += NW * TILE * 256;
    if (left == CKPT && lane == 0) *(volatile LAS unsigned*)((LAS char*)smem + LDS_CW + 32 + w * 4) = 1u;
    tile(std::false_type{});
    wait_vm<8>();
  }
  if (CKPT == 0 && lane == 0) *(volatile LAS unsigned*)((LAS char*)smem + LDS_CW + 32 + w * 4) = 1u;
  tile(std::true_type{});
  STAMP(2);

  l = xsum(l);
  {
    LAS char* dst = ringp + i * OROW + (g & 1) * 256 + (g >> 1) * 16;
#pragma unroll
    for (int cb = 0; cb < 8; ++cb) *(LAS f32x4*)(dst + cb * 32) = acc[cb];
    if (g == 0) {
      *(LAS float*)((LAS char*)smem + LDS_SM + i * 16 + w * 4) = m;
      *(LAS float*)((LAS char*)smem + LDS_SL + i * 16 + w * 4) = l;
    }
  }
  wait_lgkm();
  __builtin_amdgcn_s_barrier();
  asm volatile("" ::: "memory");
  STAMP(3);

  const f32x4 mw = *(LAS f32x4*)((LAS char*)smem + LDS_SM + qq * 16);
  const f32x4 lw = *(LAS f32x4*)((LAS char*)smem + LDS_SL + qq * 16);
  float M = fmaxf(fmaxf(mw[0], mw[1]), fmaxf(mw[2], mw[3]));
  float L = 0.f;
  f32x4 o0 = {0.f, 0.f, 0.f, 0.f}, o1 = {0.f, 0.f, 0.f, 0.f};
#pragma unroll
  for (int ww = 0; ww < NW; ++ww) {
    const float sc = __builtin_amdgcn_exp2f((mw[ww] - M) * CEXP);
    LAS char* src = (LAS char*)smem + ww * RING + qq * OROW + jj * 16;
    L = fmaf(lw[ww], sc, L);
    o0 += *(LAS f32x4*)src * sc;
    o1 += *(LAS f32x4*)(src + 256) * sc;
  }

  LAS unsigned* const cw = (LAS unsigned*)((LAS char*)smem + LDS_CW);
  unsigned* const pAck = (unsigned*)(pair + WS_ACK + w * 128);
  const unsigned a0 = cw[4 + w];
  const unsigned role = __builtin_amdgcn_readfirstlane(cw[2]);
  if (role != ROLE_COMBINE) {
    const unsigned mb = __float_as_uint(M);
    auto publish = [&](unsigned x) {
      const unsigned t = x & 3u;
      __builtin_amdgcn_raw_buffer_store_b128(tag4(o0, t), prs, tid * 16, 0, 16);
      __builtin_amdgcn_raw_buffer_store_b128(tag4(o1, t), prs, 4096 + tid * 16, 0, 16);
      if (jj == 0) {
        const u32x4 st = {(mb & 0xffff0000u) | t, (mb << 16) | t, (__float_as_uint(L) & ~3u) | t, t};
        __builtin_amdgcn_raw_buffer_store_b128(st, prs, WS_STATS + qq * 16, 0, 16);
      }
    };
    if (role == ROLE_PUBLISH) {
      publish(cw[0]);
    } else {
      const unsigned x0 = cw[1];
      const unsigned x1 = ld_agent(pC);
      publish(x1);
      if (x1 == x0) {
        for (unsigned spin = 0; spin < SPIN_LIMIT; ++spin) {
          if (ld_agent(pAck) != a0) break;
          const unsigned c = ld_agent(pC);
          if (c != x1) {
            wait_vm<0>();
            publish(c);
            break;
          }
          __builtin_amdgcn_s_sleep(8);
        }
      }
    }
  } else {
    const unsigned t = cw[0];
    for (unsigned spin = 0; spin < SPIN_LIMIT; ++spin) {
      if (tspec != 0 && __builtin_amdgcn_ballot_w64(!tagged(ra0, ra1, rst, t)) == 0) break;
      __builtin_amdgcn_s_sleep(2);
      fetchA();
      tspec = t;
    }
    if (!isP && lane == 0) st_agent(pAck, a0 + 1);
    const float MA = __uint_as_float((rst[0] & 0xffff0000u) | (rst[1] >> 16));
    combine_store(o0, o1, M, L, ra0, ra1, MA, rst[2], (char*)out + (size_t)(bh * 16 + qq) * (D * 2) + jj * 16);
  }
  STAMP(4);
}

extern "C" void kernel_launch(void* const* d_in, const int* in_sizes, int n_in, void* d_out, int out_size, void* d_ws,
                              size_t ws_size, hipStream_t stream) {
  (void)hipFuncSetAttribute((const void*)attn_fwd, hipFuncAttributeMaxDynamicSharedMemorySize, LDS_BYTES);
  attn_fwd<<<NBH * 2, NTHREADS, LDS_BYTES, stream>>>(d_in[0], d_in[1], d_in[2], d_out, (char*)d_ws);
}
```

```cpp
#include <hip/hip_runtime.h>
#include <hip/hip_bf16.h>
#include <stdint.h>

typedef __attribute__((ext_vector_type(4))) unsigned u32x4;
typedef __attribute__((ext_vector_type(2))) unsigned u32x2;
typedef __attribute__((ext_vector_type(4))) float f32x4;
typedef __attribute__((ext_vector_type(4))) short s16x4;
typedef __attribute__((ext_vector_type(8))) short s16x8;
typedef __attribute__((ext_vector_type(8))) __bf16 bf16x8;
#define LAS __attribute__((address_space(3)))
#define GAS __attribute__((address_space(1)))

constexpr int B = 16, HQ = 32, HKV = 8, SQ = 4, SK = 2048, D = 128;
constexpr int NBH = B * HKV;
constexpr int NW = 4;
constexpr int TILE = 32;
constexpr float CEXP = 0.08838834764831845f * 1.4426950408889634f;
constexpr int SLOT = 8192, RING = 2 * SLOT;
constexpr int LDS_Q = NW * RING;
constexpr int LDS_SM = LDS_Q + 4096;
constexpr int LDS_SL = LDS_SM + 256;
constexpr int LDS_CW = LDS_SL + 256;
constexpr int LDS_OLD = LDS_CW + 256;
constexpr int LDS_E = LDS_OLD + 9 * 1024;
constexpr int LDS_BYTES = 96 * 1024;
constexpr int OROW = 528;
constexpr unsigned SPIN_LIMIT = 1u << 20;
constexpr int WS_STATS = 8192;
constexpr int WS_PAYLOAD = WS_STATS + 256;
constexpr int WS_CTL = WS_PAYLOAD;
constexpr int CTL_HELLO = 0, CTL_JOIN = 2, CTL_ARR = 4;
constexpr int WS_PAIR = WS_CTL + 5 * 128;
constexpr unsigned ROLE_COMBINE = 0, ROLE_PUBLISH = 1, ROLE_UNKNOWN = 2;
static_assert((HQ / HKV) * SQ == 16, "16 packed query rows per kv head");
static_assert(LDS_E + NW * 3072 <= LDS_BYTES && 16 * OROW <= RING, "LDS layout");

__device__ __forceinline__ unsigned cvtpk(float lo, float hi) {
  unsigned r;
  asm("v_cvt_pk_bf16_f32 %0, %1, %2" : "=v"(r) : "v"(lo), "v"(hi));
  return r;
}
__device__ __forceinline__ float xmax(float x) {
  auto s = __builtin_amdgcn_permlane16_swap(__float_as_uint(x), __float_as_uint(x), false, false);
  x = fmaxf(__uint_as_float(s[0]), __uint_as_float(s[1]));
  auto t = __builtin_amdgcn_permlane32_swap(__float_as_uint(x), __float_as_uint(x), false, false);
  return fmaxf(__uint_as_float(t[0]), __uint_as_float(t[1]));
}
__device__ __forceinline__ float xsum(float x) {
  auto s = __builtin_amdgcn_permlane16_swap(__float_as_uint(x), __float_as_uint(x), false, false);
  x = __uint_as_float(s[0]) + __uint_as_float(s[1]);
  auto t = __builtin_amdgcn_permlane32_swap(__float_as_uint(x), __float_as_uint(x), false, false);
  return __uint_as_float(t[0]) + __uint_as_float(t[1]);
}
__device__ __forceinline__ bf16x8 asbf(u32x4 v) { return __builtin_bit_cast(bf16x8, v); }

__device__ __forceinline__ void dma4(unsigned m0v, __amdgpu_buffer_rsrc_t srd, unsigned soff, unsigned v0, unsigned v1,
                                     unsigned v2, unsigned v3) {
  asm volatile(
      "s_mov_b32 m0, %0\n\t"
      "s_nop 0\n\t"
      "buffer_load_dwordx4 %3, %1, %2 offen nt lds\n\t"
      "buffer_load_dwordx4 %4, %1, %2 offen offset:1024 nt lds\n\t"
      "buffer_load_dwordx4 %5, %1, %2 offen offset:2048 nt lds\n\t"
      "buffer_load_dwordx4 %6, %1, %2 offen offset:3072 nt lds"
      :
      : "s"(m0v), "s"(srd), "s"(soff), "v"(v0), "v"(v1), "v"(v2), "v"(v3)
      : "memory");
}
__device__ __forceinline__ void dma1(unsigned m0v, __amdgpu_buffer_rsrc_t srd, unsigned soff, unsigned v0) {
  asm volatile(
      "s_mov_b32 m0, %0\n\t"
      "s_nop 0\n\t"
      "buffer_load_dwordx4 %3, %1, %2 offen nt lds"
      :
      : "s"(m0v), "s"(srd), "s"(soff), "v"(v0)
      : "memory");
}
template <int N>
__device__ __forceinline__ void wait_vm() { asm volatile("s_waitcnt vmcnt(%0)" ::"i"(N) : "memory"); }
__device__ __forceinline__ void wait_lgkm() { asm volatile("s_waitcnt lgkmcnt(0)" ::: "memory"); }

__device__ __forceinline__ unsigned ld_agent(const unsigned* p) { return __hip_atomic_load((GAS const unsigned*)p, __ATOMIC_RELAXED, __HIP_MEMORY_SCOPE_AGENT); }
__device__ __forceinline__ void st_agent(unsigned* p, unsigned v) { __hip_atomic_store((GAS unsigned*)p, v, __ATOMIC_RELAXED, __HIP_MEMORY_SCOPE_AGENT); }
__device__ __forceinline__ u32x4 asu(f32x4 x) { return __builtin_bit_cast(u32x4, x); }
__device__ __forceinline__ u32x4 flip4(u32x4 u, u32x4 old) {
  return u32x4{(u[0] & ~1u) | (~old[0] & 1u), (u[1] & ~1u) | (~old[1] & 1u), (u[2] & ~1u) | (~old[2] & 1u), (u[3] & ~1u) | (~old[3] & 1u)};
}
__device__ __forceinline__ unsigned changed4(u32x4 a, u32x4 old) { return ((a[0] ^ old[0]) & (a[1] ^ old[1])) & ((a[2] ^ old[2]) & (a[3] ^ old[3])); }

__device__ __forceinline__ f32x4 clr0(u32x4 u) { return __builtin_bit_cast(f32x4, u & ~1u); }
__device__ __forceinline__ float max4(f32x4 x) { return fmaxf(fmaxf(x[0], x[1]), fmaxf(x[2], x[3])); }

constexpr int NTHREADS = (NW + 1) * 64;
constexpr int NX = 16;
constexpr int AHEAD = 4;
constexpr int CKPT = 1;
constexpr int POLL0 = 32, POLL1 = 16;
constexpr unsigned KV_BYTES = (unsigned)B * HKV * SK * D * 2, Q_BYTES = (unsigned)B * HQ * SQ * D * 2;

__global__ __launch_bounds__(NTHREADS) void attn_fwd(const void* __restrict__ q, const void* __restrict__ k, const void* __restrict__ v,
                                                     void* __restrict__ out, char* __restrict__ ws) {
  extern __shared__ __attribute__((aligned(16))) char smem[];
  const int tid = threadIdx.x, lane = tid & 63;
  const int w = __builtin_amdgcn_readfirstlane(tid >> 6);
  const int x8 = blockIdx.x & 7;
  const bool isP = x8 < 4;
  const int bh = (blockIdx.x >> 3) * 4 + (isP ? x8 : (x8 ^ 5));
  const int i = lane & 15, g = lane >> 4;
  const unsigned lds0 = (unsigned)(size_t)(LAS char*)smem;

  if (w == NW) {
    asm volatile("" ::"s"(q), "s"(k), "s"(v), "s"(out));
    char* const pair = ws + (size_t)bh * WS_PAIR;
    const auto prs = __builtin_amdgcn_make_buffer_rsrc((void*)pair, 0, WS_PAYLOAD, 0x00020000);
    LAS unsigned* const cw = (LAS unsigned*)((LAS char*)smem + LDS_CW);
    unsigned* const ctl = (unsigned*)(pair + WS_CTL);
    const int me = isP ? 1 : 0;
    if (lane < 12) cw[lane] = lane == 2 ? ROLE_UNKNOWN : 0u;
    unsigned cv;
    {
      const unsigned vo = lane * 16, vo4 = 4096 + lane * 16, vo8 = 8192 + lane * 16;
      asm volatile(
          "global_load_dword %0, %1, off sc1\n\t"
          "s_mov_b32 m0, %2\n\t"
          "s_nop 0\n\t"
          "buffer_load_dwordx4 %6, %5, 0 offen sc1 nt lds\n\t"
          "buffer_load_dwordx4 %6, %5, 0 offen offset:1024 sc1 nt lds\n\t"
          "buffer_load_dwordx4 %6, %5, 0 offen offset:2048 sc1 nt lds\n\t"
          "buffer_load_dwordx4 %6, %5, 0 offen offset:3072 sc1 nt lds\n\t"
          "s_mov_b32 m0, %3\n\t"
          "s_nop 0\n\t"
          "buffer_load_dwordx4 %7, %5, 0 offen sc1 nt lds\n\t"
          "buffer_load_dwordx4 %7, %5, 0 offen offset:1024 sc1 nt lds\n\t"
          "buffer_load_dwordx4 %7, %5, 0 offen offset:2048 sc1 nt lds\n\t"
          "buffer_load_dwordx4 %7, %5, 0 offen offset:3072 sc1 nt lds\n\t"
          "s_mov_b32 m0, %4\n\t"
          "s_nop 0\n\t"
          "buffer_load_dwordx4 %8, %5, 0 offen sc1 nt lds\n\t"
          "s_waitcnt lgkmcnt(0)\n\t"
          "s_barrier\n\t"
          "s_waitcnt vmcnt(0)"
          : "=&v"(cv)
          : "v"(ctl + min(lane, 4) * 32), "s"(lds0 + LDS_OLD), "s"(lds0 + LDS_OLD + 4096), "s"(lds0 + LDS_OLD + 8192), "s"(prs), "v"(vo), "v"(vo4), "v"(vo8)
          : "memory");
    }
    const unsigned hello_mine0 = __builtin_amdgcn_readlane(cv, CTL_HELLO + me), hello_peer0 = __builtin_amdgcn_readlane(cv, CTL_HELLO + 1 - me);
    const unsigned join_peer0 = __builtin_amdgcn_readlane(cv, CTL_JOIN + 1 - me), arr0 = __builtin_amdgcn_readlane(cv, CTL_ARR);
    unsigned hello = hello_mine0 + 1;
    if (hello == join_peer0) ++hello;
    if (lane == 0) st_agent(ctl + (CTL_HELLO + me) * 32, hello);
    bool dyn = false;
    const unsigned* const pollp = ctl + ((lane & 1) ? CTL_JOIN + 1 - me : CTL_HELLO + 1 - me) * 32;
    __builtin_amdgcn_s_sleep(POLL0);
    for (unsigned spin = 0; spin < SPIN_LIMIT; ++spin) {
      if (!dyn) {
        unsigned x = 0;
        if (lane < 2) x = ld_agent(pollp);
        const unsigned xh = __builtin_amdgcn_readlane(x, 0), xj = __builtin_amdgcn_readlane(x, 1);
        if (xh != hello_peer0) {
          dyn = true;
          if (lane == 0) st_agent(ctl + (CTL_JOIN + me) * 32, xh);
        } else if (xj == hello) {
          dyn = true;
        }
        if (!dyn) { __builtin_amdgcn_s_sleep(POLL1); continue; }
      }
      const u32x4 f = *(volatile LAS u32x4*)(cw + 8);
      if ((f[0] & f[1] & f[2] & f[3]) != 0) break;
      __builtin_amdgcn_s_sleep(4);
    }
    unsigned old = 0;
    if (lane == 0) old = __hip_atomic_fetch_add((GAS unsigned*)(ctl + CTL_ARR * 32), 1u, __ATOMIC_RELAXED, __HIP_MEMORY_SCOPE_AGENT);
    const unsigned role = __builtin_amdgcn_readfirstlane(old) == arr0 ? ROLE_PUBLISH : ROLE_COMBINE;
    if (lane == 0) *(volatile LAS unsigned*)(cw + 2) = role;
    wait_lgkm();
    return;
  }

  const int nr = 8 - (blockIdx.x & 1);
  const unsigned bhoff = (unsigned)bh * (SK * D * 2);
  const unsigned ring0 = bhoff + (isP ? 0 : SK - nr * NW * TILE) * (D * 2);
  const unsigned ext0 = bhoff + ((isP ? nr * NW * TILE : SK - nr * NW * TILE - NW * NX) + w * NX) * (D * 2);
  const unsigned ring = lds0 + w * RING;

  unsigned kv[4], vv[2];
#pragma unroll
  for (int j = 0; j < 4; ++j) kv[j] = g * 256 + ((i ^ (4 * j + g)) * 16);
#pragma unroll
  for (int j = 0; j < 2; ++j) vv[j] = g * 256 + ((i ^ (8 * j + 2 * g)) * 16);
  const unsigned qv = w * 1024 + g * 256 + ((i ^ (4 * w + g)) * 16);
  const unsigned qso = (unsigned)bh * (16 * D * 2), qm0 = lds0 + LDS_Q + w * 1024, vm0 = ring + SLOT;

  asm volatile("" ::"v"(kv[0]), "v"(kv[1]), "v"(kv[2]), "v"(kv[3]), "v"(vv[0]), "v"(vv[1]), "v"(qv), "s"(qso), "s"(qm0), "s"(vm0), "s"(ring), "s"(ext0), "s"(ring0));
  asm volatile("" : "+s"(q), "+s"(k), "+s"(v));
  const auto krs = __builtin_amdgcn_make_buffer_rsrc((void*)k, 0, KV_BYTES, 0x00020000);
  const auto vrs = __builtin_amdgcn_make_buffer_rsrc((void*)v, 0, KV_BYTES, 0x00020000);
  const auto qrs = __builtin_amdgcn_make_buffer_rsrc((void*)q, 0, Q_BYTES, 0x00020000);

  auto issueK = [&](unsigned o) {
    dma4(ring, krs, o, kv[0], kv[1], kv[2], kv[3]);
    dma4(ring + 4096, krs, o + 4096, kv[0], kv[1], kv[2], kv[3]);
  };
  auto issueV = [&](unsigned o) {
    dma4(ring + SLOT, vrs, o, vv[0], vv[1], vv[0], vv[1]);
    dma4(ring + SLOT + 4096, vrs, o + 4096, vv[0], vv[1], vv[0], vv[1]);
  };
  auto tile_off = [&](int r) -> unsigned { return ring0 + (unsigned)(NW * r + ((w + r) & (NW - 1))) * (TILE * 256); };

  dma1(qm0, qrs, qso, qv);
  dma4(ring, krs, ext0, kv[0], kv[1], kv[2], kv[3]);
  dma4(vm0, vrs, ext0, vv[0], vv[1], vv[0], vv[1]);
  asm volatile("" : "+s"(ws), "+s"(out));
  const auto prs = __builtin_amdgcn_make_buffer_rsrc((void*)(ws + (size_t)bh * WS_PAIR), 0, WS_PAYLOAD, 0x00020000);

  LAS char* const ringp = (LAS char*)smem + w * RING;
  unsigned ka[4];
#pragma unroll
  for (int s = 0; s < 4; ++s) ka[s] = i * 256 + (((4 * s) ^ g ^ i) * 16);
  const int q_ = i >> 2, p_ = i & 3;
  const unsigned va0 = (4 * g + q_) * 256 + (((8 * (g & 1) + 2 * q_) + (p_ >> 1)) * 16) + (p_ & 1) * 8;

  wait_vm<8>();
  __builtin_amdgcn_s_barrier();
  asm volatile("" ::: "memory");
  u32x4 Qf[4];
#pragma unroll
  for (int s = 0; s < 4; ++s) Qf[s] = *(LAS u32x4*)((LAS char*)smem + LDS_Q + ka[s]);

  f32x4 acc[8];
  float m, l;
  unsigned cur = tile_off(0);

  {
    u32x4 kf[4];
    wait_vm<4>();
#pragma unroll
    for (int s = 0; s < 4; ++s) kf[s] = *(LAS u32x4*)(ringp + ka[s]);
    wait_lgkm();
    __builtin_amdgcn_sched_barrier(0);
    issueK(cur);
    __builtin_amdgcn_sched_barrier(0);
    f32x4 s0 = {0.f, 0.f, 0.f, 0.f};
#pragma unroll
    for (int s = 0; s < 4; ++s) s0 = __builtin_amdgcn_mfma_f32_16x16x32_bf16(asbf(kf[s]), asbf(Qf[s]), s0, 0, 0, 0);
    m = xmax(max4(s0));
    const float mc = -m * CEXP;
    float p[4];
#pragma unroll
    for (int r = 0; r < 4; ++r) p[r] = __builtin_amdgcn_exp2f(fmaf(s0[r], CEXP, mc));
    l = (p[0] + p[1]) + (p[2] + p[3]);
    const u32x2 P = {cvtpk(p[0], p[1]), cvtpk(p[2], p[3])};
    __builtin_amdgcn_sched_barrier(0);
    wait_vm<8>();
    __builtin_amdgcn_sched_barrier(0);
    s16x4 vf[8];
#pragma unroll
    for (int cb = 0; cb < 8; ++cb) vf[cb] = __builtin_amdgcn_ds_read_tr16_b64_v4i16((LAS s16x4*)(ringp + SLOT + (va0 ^ (cb << 5))));
    wait_lgkm();
    __builtin_amdgcn_sched_barrier(0);
#pragma unroll
    for (int cb = 0; cb < 8; ++cb)
      acc[cb] = __builtin_amdgcn_mfma_f32_16x16x16bf16_1k(vf[cb], __builtin_bit_cast(s16x4, P), f32x4{0.f, 0.f, 0.f, 0.f}, 0, 0, 0);
  }

  auto scores = [&](f32x4& s0, f32x4& s1) {
    u32x4 kf[8];
#pragma unroll
    for (int s = 0; s < 4; ++s) {
      kf[2 * s] = *(LAS u32x4*)(ringp + ka[s]);
      kf[2 * s + 1] = *(LAS u32x4*)(ringp + 4096 + ka[s]);
    }
    wait_lgkm();
    __builtin_amdgcn_sched_barrier(0);
    s0 = f32x4{0.f, 0.f, 0.f, 0.f};
    s1 = s0;
#pragma unroll
    for (int s = 0; s < 4; ++s) {
      s0 = __builtin_amdgcn_mfma_f32_16x16x32_bf16(asbf(kf[2 * s]), asbf(Qf[s]), s0, 0, 0, 0);
      s1 = __builtin_amdgcn_mfma_f32_16x16x32_bf16(asbf(kf[2 * s + 1]), asbf(Qf[s]), s1, 0, 0, 0);
    }
  };
  auto probs = [&](f32x4 s0, f32x4 s1) {
    const float mc = -m * CEXP;
    float p[8];
#pragma unroll
    for (int r = 0; r < 4; ++r) {
      p[r] = __builtin_amdgcn_exp2f(fmaf(s0[r], CEXP, mc));
      p[4 + r] = __builtin_amdgcn_exp2f(fmaf(s1[r], CEXP, mc));
    }
    l += ((p[0] + p[1]) + (p[2] + p[3])) + ((p[4] + p[5]) + (p[6] + p[7]));
    return u32x4{cvtpk(p[0], p[1]), cvtpk(p[2], p[3]), cvtpk(p[4], p[5]), cvtpk(p[6], p[7])};
  };
  auto rescale = [&](float mn) {
    const float alpha = __builtin_amdgcn_exp2f((m - mn) * CEXP);
    l *= alpha;
#pragma unroll
    for (int e = 0; e < 8; ++e) acc[e] *= alpha;
    m = mn;
  };
  auto values = [&](u32x4 P, bool drain) {
    s16x4 vf[16];
#pragma unroll
    for (int cb = 0; cb < 8; ++cb) {
      const unsigned a = va0 ^ (cb << 5);
      vf[2 * cb] = __builtin_amdgcn_ds_read_tr16_b64_v4i16((LAS s16x4*)(ringp + SLOT + a));
      vf[2 * cb + 1] = __builtin_amdgcn_ds_read_tr16_b64_v4i16((LAS s16x4*)(ringp + SLOT + 4096 + a));
    }
    if (drain) wait_lgkm();
    __builtin_amdgcn_sched_barrier(0);
#pragma unroll
    for (int cb = 0; cb < 8; ++cb) {
      const s16x8 f = __builtin_shufflevector(vf[2 * cb], vf[2 * cb + 1], 0, 1, 2, 3, 4, 5, 6, 7);
      acc[cb] = __builtin_amdgcn_mfma_f32_16x16x32_bf16(__builtin_bit_cast(bf16x8, f), asbf(P), acc[cb], 0, 0, 0);
    }
  };

#pragma unroll 1
  for (int r = 1; r < nr; ++r) {
    if (nr - r == CKPT && lane == 0) *(volatile LAS unsigned*)((LAS char*)smem + LDS_CW + 32 + w * 4) = 1u;
    wait_vm<AHEAD>();
    issueV(cur);
    wait_vm<8>();
    __builtin_amdgcn_sched_barrier(0);
    f32x4 s0, s1;
    scores(s0, s1);
    const float mn = fmaxf(m, xmax(fmaxf(max4(s0), max4(s1))));
    if (__builtin_amdgcn_ballot_w64(mn > m)) rescale(mn);
    const u32x4 P = probs(s0, s1);
    cur = tile_off(r);
    __builtin_amdgcn_sched_barrier(0);
    wait_vm<AHEAD>();
    issueK(cur);
    wait_vm<8>();
    __builtin_amdgcn_sched_barrier(0);
    values(P, true);
  }

  const int qq = tid >> 4, jj = tid & 15;
  LAS char* const sm = (LAS char*)smem;
  u32x4 ra0 = {0u, 0u, 0u, 0u}, ra1 = ra0, rst = ra0;
  f32x4 ta0 = {0.f, 0.f, 0.f, 0.f}, ta1 = ta0;
  float fb = 0.f, tl = 0.f;
  bool have = false;
  auto readRole = [&]() { return __builtin_amdgcn_readfirstlane(*(volatile LAS unsigned*)(sm + LDS_CW + 8)); };
  auto fetchLds = [&]() {
    const unsigned o0 = tid * 16, o1 = 4096 + tid * 16, o2 = WS_STATS + qq * 16, mb = lds0 + LDS_E + w * 3072;
    asm volatile(
        "s_mov_b32 m0, %0\n\t"
        "s_nop 0\n\t"
        "buffer_load_dwordx4 %4, %3, 0 offen sc1 lds\n\t"
        "s_mov_b32 m0, %1\n\t"
        "s_nop 0\n\t"
        "buffer_load_dwordx4 %5, %3, 0 offen sc1 lds\n\t"
        "s_mov_b32 m0, %2\n\t"
        "s_nop 0\n\t"
        "buffer_load_dwordx4 %6, %3, 0 offen sc1 lds"
        :
        : "s"(mb), "s"(mb + 1024), "s"(mb + 2048), "s"(prs), "v"(o0), "v"(o1), "v"(o2)
        : "memory");
  };

  unsigned role = readRole();
  wait_vm<AHEAD>();
  const bool early = role == ROLE_COMBINE;
  if (early) fetchLds();
  issueV(cur);
  if (early) wait_vm<11>(); else wait_vm<8>();
  __builtin_amdgcn_sched_barrier(0);
  f32x4 s0, s1;
  scores(s0, s1);
  const float mn = fmaxf(m, xmax(fmaxf(max4(s0), max4(s1))));
  if (g == 0) *(LAS float*)(sm + LDS_SM + i * 16 + w * 4) = mn;
  wait_lgkm();
  __builtin_amdgcn_s_barrier();
  asm volatile("" ::: "memory");
  if (role == ROLE_UNKNOWN) role = readRole();
  const bool late = role == ROLE_COMBINE && !early;
  if (late) fetchLds();
  const f32x4 mw = *(LAS f32x4*)(sm + LDS_SM + i * 16), mq = *(LAS f32x4*)(sm + LDS_SM + qq * 16);
  const u32x4 old0 = *(LAS u32x4*)(sm + LDS_OLD + tid * 16), old1 = *(LAS u32x4*)(sm + LDS_OLD + 4096 + tid * 16);
  const u32x4 olds = *(LAS u32x4*)(sm + LDS_OLD + WS_STATS + qq * 16);
  rescale(max4(mw));
#pragma unroll
  for (int e = 0; e < 8; ++e) asm volatile("" : "+v"(acc[e]));
  const u32x4 P = probs(s0, s1);
  l = xsum(l);
  if (g == 0) *(LAS float*)(sm + LDS_SL + i * 16 + w * 4) = l;
  const float M = max4(mq);

  auto arrived = [&]() {
    const unsigned ch = changed4(ra0, old0) & changed4(ra1, old1) & changed4(rst, olds);
    return __builtin_amdgcn_ballot_w64((ch & 1u) == 0) == 0;
  };
  auto prepare = [&]() {
#pragma clang fp contract(off)
    const float MA = __uint_as_float((rst[0] & 0xffff0000u) | (rst[1] >> 16));
    const float Mt = fmaxf(M, MA);
    fb = __builtin_amdgcn_exp2f((M - Mt) * CEXP);
    const float fa = __builtin_amdgcn_exp2f((MA - Mt) * CEXP);
    tl = __uint_as_float(rst[2] & ~1u) * fa;
    ta0 = clr0(ra0) * fa;
    ta1 = clr0(ra1) * fa;
  };
  auto takeFetched = [&]() {
    LAS char* e = sm + LDS_E + w * 3072 + lane * 16;
    ra0 = *(LAS u32x4*)e;
    ra1 = *(LAS u32x4*)(e + 1024);
    rst = *(LAS u32x4*)(e + 2048);
    have = arrived();
    if (have) prepare();
  };
  GAS char* dst = (GAS char*)out + (size_t)(bh * 16 + qq) * (D * 2) + jj * 16;
  const unsigned mb = __float_as_uint(M);
  u32x4 stw = flip4(u32x4{mb & 0xffff0000u, mb << 16, 0u, 0u}, olds);
  unsigned wr = ring + i * OROW + (g & 1) * 256 + (g >> 1) * 16, rd = lds0 + qq * OROW + jj * 16, rl = lds0 + LDS_SL + qq * 16;
  unsigned so0 = tid * 16, so1 = 4096 + tid * 16, so2 = WS_STATS + qq * 16;
  asm volatile("" : "+v"(stw), "+v"(dst), "+v"(wr), "+v"(rd), "+v"(rl), "+v"(so0), "+v"(so1), "+v"(so2));
  if (early) { wait_vm<8>(); takeFetched(); }
  __builtin_amdgcn_sched_barrier(0);
  if (late) wait_vm<3>(); else wait_vm<0>();
  __builtin_amdgcn_sched_barrier(0);
  values(P, false);
  {
    LAS char* d = (LAS char*)(size_t)wr;
#pragma unroll
    for (int cb = 0; cb < 8; ++cb) *(LAS f32x4*)(d + cb * 32) = acc[cb];
  }
  if (late) { wait_vm<0>(); takeFetched(); }
  wait_lgkm();
  __builtin_amdgcn_s_barrier();
  asm volatile("" ::: "memory");

  const f32x4 lw = *(LAS f32x4*)(size_t)rl;
  f32x4 pa[NW], pb[NW];
#pragma unroll
  for (int ww = 0; ww < NW; ++ww) {
    LAS char* src = (LAS char*)(size_t)rd + ww * RING;
    pa[ww] = *(LAS f32x4*)src;
    pb[ww] = *(LAS f32x4*)(src + 256);
  }
  const float L = (lw[0] + lw[1]) + (lw[2] + lw[3]);
  const f32x4 o0 = (pa[0] + pa[1]) + (pa[2] + pa[3]), o1 = (pb[0] + pb[1]) + (pb[2] + pb[3]);

  if (role != ROLE_COMBINE) {
    __builtin_amdgcn_raw_buffer_store_b128(flip4(asu(o0), old0), prs, so0, 0, 16);
    __builtin_amdgcn_raw_buffer_store_b128(flip4(asu(o1), old1), prs, so1, 0, 16);
    if (jj == 0) {
      stw[2] |= __float_as_uint(L) & ~1u;
      __builtin_amdgcn_raw_buffer_store_b128(stw, prs, so2, 0, 16);
    }
  } else {
    if (!have) {
      for (unsigned spin = 0; spin < SPIN_LIMIT; ++spin) {
        ra0 = __builtin_amdgcn_raw_buffer_load_b128(prs, so0, 0, 16);
        ra1 = __builtin_amdgcn_raw_buffer_load_b128(prs, so1, 0, 16);
        rst = __builtin_amdgcn_raw_buffer_load_b128(prs, so2, 0, 16);
        if (arrived()) break;
        __builtin_amdgcn_s_sleep(2);
      }
      prepare();
    }
    {
#pragma clang fp contract(off)
      const float inv = __builtin_amdgcn_rcpf(__uint_as_float(__float_as_uint(L) & ~1u) * fb + tl);
      const f32x4 r0 = (clr0(asu(o0)) * fb + ta0) * inv, r1 = (clr0(asu(o1)) * fb + ta1) * inv;
      const u32x4 o = {cvtpk(r0[0], r0[1]), cvtpk(r0[2], r0[3]), cvtpk(r1[0], r1[1]), cvtpk(r1[2], r1[3])};
      *(GAS u32x4*)dst = o;
    }
  }
}

extern "C" void kernel_launch(void* const* d_in, const int* in_sizes, int n_in, void* d_out, int out_size, void* d_ws,
                              size_t ws_size, hipStream_t stream) {
  (void)hipFuncSetAttribute((const void*)attn_fwd, hipFuncAttributeMaxDynamicSharedMemorySize, LDS_BYTES);
  attn_fwd<<<NBH * 2, NTHREADS, LDS_BYTES, stream>>>(d_in[0], d_in[1], d_in[2], d_out, (char*)d_ws);
}
```

```cpp
#include <hip/hip_runtime.h>
#include <hip/hip_bf16.h>
#include <stdint.h>

typedef __attribute__((ext_vector_type(4))) unsigned u32x4;
typedef __attribute__((ext_vector_type(2))) unsigned u32x2;
typedef __attribute__((ext_vector_type(4))) float f32x4;
typedef __attribute__((ext_vector_type(4))) short s16x4;
typedef __attribute__((ext_vector_type(8))) short s16x8;
typedef __attribute__((ext_vector_type(8))) __bf16 bf16x8;
#define LAS __attribute__((address_space(3)))
#define GAS __attribute__((address_space(1)))

constexpr int B = 16, HQ = 32, HKV = 8, SQ = 4, SK = 2048, D = 128;
constexpr int NBH = B * HKV;
constexpr int NW = 4;
constexpr int TILE = 32;
constexpr float CEXP = 0.08838834764831845f * 1.4426950408889634f;
constexpr int SLOT = 8192, RING = 2 * SLOT;
constexpr int LDS_Q = NW * RING;
constexpr int LDS_SM = LDS_Q + 4096;
constexpr int LDS_SL = LDS_SM + 256;
constexpr int LDS_CW = LDS_SL + 256;
constexpr int LDS_OLD = LDS_CW + 256;
constexpr int LDS_E = LDS_OLD + 9 * 1024;
constexpr int LDS_BYTES = 96 * 1024;
constexpr int OROW = 528;
constexpr unsigned SPIN_LIMIT = 1u << 20;
constexpr int WS_STATS = 8192;
constexpr int WS_PAYLOAD = WS_STATS + 256;
constexpr int WS_CTL = WS_PAYLOAD;
constexpr int CTL_HELLO = 0, CTL_JOIN = 2, CTL_ARR = 4;
constexpr int WS_PAIR = WS_CTL + 5 * 128;
constexpr unsigned ROLE_COMBINE = 0, ROLE_PUBLISH = 1, ROLE_UNKNOWN = 2;
static_assert((HQ / HKV) * SQ == 16, "16 packed query rows per kv head");
static_assert(LDS_E + NW * 3072 <= LDS_BYTES && 16 * OROW <= RING, "LDS layout");

__device__ __forceinline__ unsigned cvtpk(float lo, float hi) {
  unsigned r;
  asm("v_cvt_pk_bf16_f32 %0, %1, %2" : "=v"(r) : "v"(lo), "v"(hi));
  return r;
}
__device__ __forceinline__ float xmax(float x) {
  auto s = __builtin_amdgcn_permlane16_swap(__float_as_uint(x), __float_as_uint(x), false, false);
  x = fmaxf(__uint_as_float(s[0]), __uint_as_float(s[1]));
  auto t = __builtin_amdgcn_permlane32_swap(__float_as_uint(x), __float_as_uint(x), false, false);
  return fmaxf(__uint_as_float(t[0]), __uint_as_float(t[1]));
}
__device__ __forceinline__ float xsum(float x) {
  auto s = __builtin_amdgcn_permlane16_swap(__float_as_uint(x), __float_as_uint(x), false, false);
  x = __uint_as_float(s[0]) + __uint_as_float(s[1]);
  auto t = __builtin_amdgcn_permlane32_swap(__float_as_uint(x), __float_as_uint(x), false, false);
  return __uint_as_float(t[0]) + __uint_as_float(t[1]);
}
__device__ __forceinline__ bf16x8 asbf(u32x4 v) { return __builtin_bit_cast(bf16x8, v); }

__device__ __forceinline__ void dma4(unsigned m0v, __amdgpu_buffer_rsrc_t srd, unsigned soff, unsigned v0, unsigned v1,
                                     unsigned v2, unsigned v3) {
  asm volatile(
      "s_mov_b32 m0, %0\n\t"
      "s_nop 0\n\t"
      "buffer_load_dwordx4 %3, %1, %2 offen nt lds\n\t"
      "buffer_load_dwordx4 %4, %1, %2 offen offset:1024 nt lds\n\t"
      "buffer_load_dwordx4 %5, %1, %2 offen offset:2048 nt lds\n\t"
      "buffer_load_dwordx4 %6, %1, %2 offen offset:3072 nt lds"
      :
      : "s"(m0v), "s"(srd), "s"(soff), "v"(v0), "v"(v1), "v"(v2), "v"(v3)
      : "memory");
}
__device__ __forceinline__ void dma1(unsigned m0v, __amdgpu_buffer_rsrc_t srd, unsigned soff, unsigned v0) {
  asm volatile(
      "s_mov_b32 m0, %0\n\t"
      "s_nop 0\n\t"
      "buffer_load_dwordx4 %3, %1, %2 offen nt lds"
      :
      : "s"(m0v), "s"(srd), "s"(soff), "v"(v0)
      : "memory");
}
template <int N>
__device__ __forceinline__ void wait_vm() { asm volatile("s_waitcnt vmcnt(%0)" ::"i"(N) : "memory"); }
__device__ __forceinline__ void wait_lgkm() { asm volatile("s_waitcnt lgkmcnt(0)" ::: "memory"); }

__device__ __forceinline__ unsigned ld_agent(const unsigned* p) { return __hip_atomic_load((GAS const unsigned*)p, __ATOMIC_RELAXED, __HIP_MEMORY_SCOPE_AGENT); }
__device__ __forceinline__ void st_agent(unsigned* p, unsigned v) { __hip_atomic_store((GAS unsigned*)p, v, __ATOMIC_RELAXED, __HIP_MEMORY_SCOPE_AGENT); }
__device__ __forceinline__ u32x4 asu(f32x4 x) { return __builtin_bit_cast(u32x4, x); }
__device__ __forceinline__ u32x4 flip4(u32x4 u, u32x4 old) {
  return u32x4{(u[0] & ~1u) | (~old[0] & 1u), (u[1] & ~1u) | (~old[1] & 1u), (u[2] & ~1u) | (~old[2] & 1u), (u[3] & ~1u) | (~old[3] & 1u)};
}
__device__ __forceinline__ unsigned changed4(u32x4 a, u32x4 old) { return ((a[0] ^ old[0]) & (a[1] ^ old[1])) & ((a[2] ^ old[2]) & (a[3] ^ old[3])); }

__device__ __forceinline__ f32x4 clr0(u32x4 u) { return __builtin_bit_cast(f32x4, u & ~1u); }
__device__ __forceinline__ float max4(f32x4 x) { return fmaxf(fmaxf(x[0], x[1]), fmaxf(x[2], x[3])); }

constexpr int NTHREADS = (NW + 1) * 64;
constexpr int NX = 16;
constexpr int AHEAD = 4;
constexpr int CKPT = 1;
constexpr int POLL0 = 32, POLL1 = 16;
constexpr unsigned KV_BYTES = (unsigned)B * HKV * SK * D * 2, Q_BYTES = (unsigned)B * HQ * SQ * D * 2;

__global__ __launch_bounds__(NTHREADS) void attn_fwd(const void* __restrict__ q, const void* __restrict__ k, const void* __restrict__ v,
                                                     void* __restrict__ out, char* __restrict__ ws) {
  extern __shared__ __attribute__((aligned(16))) char smem[];
  const unsigned long long tstart = __builtin_amdgcn_s_memrealtime();
  const int tid = threadIdx.x, lane = tid & 63;
  const int w = __builtin_amdgcn_readfirstlane(tid >> 6);
  const int x8 = blockIdx.x & 7;
  const bool isP = x8 < 4;
  const int bh = (blockIdx.x >> 3) * 4 + (isP ? x8 : (x8 ^ 5));
  const int i = lane & 15, g = lane >> 4;
  const unsigned lds0 = (unsigned)(size_t)(LAS char*)smem;

  if (w == NW) {
    asm volatile("" ::"s"(q), "s"(k), "s"(v), "s"(out));
    char* const pair = ws + (size_t)bh * WS_PAIR;
    const auto prs = __builtin_amdgcn_make_buffer_rsrc((void*)pair, 0, WS_PAYLOAD, 0x00020000);
    LAS unsigned* const cw = (LAS unsigned*)((LAS char*)smem + LDS_CW);
    unsigned* const ctl = (unsigned*)(pair + WS_CTL);
    const int me = isP ? 1 : 0;
    if (lane < 12) cw[lane] = lane == 2 ? ROLE_UNKNOWN : 0u;
    unsigned cv;
    {
      const unsigned vo = lane * 16, vo4 = 4096 + lane * 16, vo8 = 8192 + lane * 16;
      asm volatile(
          "global_load_dword %0, %1, off sc1\n\t"
          "s_mov_b32 m0, %2\n\t"
          "s_nop 0\n\t"
          "buffer_load_dwordx4 %6, %5, 0 offen sc1 nt lds\n\t"
          "buffer_load_dwordx4 %6, %5, 0 offen offset:1024 sc1 nt lds\n\t"
          "buffer_load_dwordx4 %6, %5, 0 offen offset:2048 sc1 nt lds\n\t"
          "buffer_load_dwordx4 %6, %5, 0 offen offset:3072 sc1 nt lds\n\t"
          "s_mov_b32 m0, %3\n\t"
          "s_nop 0\n\t"
          "buffer_load_dwordx4 %7, %5, 0 offen sc1 nt lds\n\t"
          "buffer_load_dwordx4 %7, %5, 0 offen offset:1024 sc1 nt lds\n\t"
          "buffer_load_dwordx4 %7, %5, 0 offen offset:2048 sc1 nt lds\n\t"
          "buffer_load_dwordx4 %7, %5, 0 offen offset:3072 sc1 nt lds\n\t"
          "s_mov_b32 m0, %4\n\t"
          "s_nop 0\n\t"
          "buffer_load_dwordx4 %8, %5, 0 offen sc1 nt lds\n\t"
          "s_waitcnt lgkmcnt(0)\n\t"
          "s_barrier\n\t"
          "s_waitcnt vmcnt(0)"
          : "=&v"(cv)
          : "v"(ctl + min(lane, 4) * 32), "s"(lds0 + LDS_OLD), "s"(lds0 + LDS_OLD + 4096), "s"(lds0 + LDS_OLD + 8192), "s"(prs), "v"(vo), "v"(vo4), "v"(vo8)
          : "memory");
    }
    const unsigned hello_mine0 = __builtin_amdgcn_readlane(cv, CTL_HELLO + me), hello_peer0 = __builtin_amdgcn_readlane(cv, CTL_HELLO + 1 - me);
    const unsigned join_peer0 = __builtin_amdgcn_readlane(cv, CTL_JOIN + 1 - me), arr0 = __builtin_amdgcn_readlane(cv, CTL_ARR);
    unsigned hello = hello_mine0 + 1;
    if (hello == join_peer0) ++hello;
    if (lane == 0) st_agent(ctl + (CTL_HELLO + me) * 32, hello);
    bool dyn = false;
    const unsigned* const pollp = ctl + ((lane & 1) ? CTL_JOIN + 1 - me : CTL_HELLO + 1 - me) * 32;
    __builtin_amdgcn_s_sleep(POLL0);
    for (unsigned spin = 0; spin < SPIN_LIMIT; ++spin) {
      if (!dyn) {
        unsigned x = 0;
        if (lane < 2) x = ld_agent(pollp);
        const unsigned xh = __builtin_amdgcn_readlane(x, 0), xj = __builtin_amdgcn_readlane(x, 1);
        if (xh != hello_peer0) {
          dyn = true;
          if (lane == 0) st_agent(ctl + (CTL_JOIN + me) * 32, xh);
        } else if (xj == hello) {
          dyn = true;
        }
        if (!dyn) { __builtin_amdgcn_s_sleep(POLL1); continue; }
      }
      const u32x4 f = *(volatile LAS u32x4*)(cw + 8);
      if ((f[0] & f[1] & f[2] & f[3]) != 0) break;
      __builtin_amdgcn_s_sleep(4);
    }
    unsigned old = 0;
    if (lane == 0) old = __hip_atomic_fetch_add((GAS unsigned*)(ctl + CTL_ARR * 32), 1u, __ATOMIC_RELAXED, __HIP_MEMORY_SCOPE_AGENT);
    const unsigned role = __builtin_amdgcn_readfirstlane(old) == arr0 ? ROLE_PUBLISH : ROLE_COMBINE;
    if (lane == 0) *(volatile LAS unsigned*)(cw + 2) = role;
    wait_lgkm();
    return;
  }

  const int nr = 8 - (blockIdx.x & 1);
  const unsigned bhoff = (unsigned)bh * (SK * D * 2);
  const unsigned ring0 = bhoff + (isP ? 0 : SK - nr * NW * TILE) * (D * 2);
  const unsigned ext0 = bhoff + ((isP ? nr * NW * TILE : SK - nr * NW * TILE - NW * NX) + w * NX) * (D * 2);
  const unsigned ring = lds0 + w * RING;

  unsigned kv[4], vv[2];
#pragma unroll
  for (int j = 0; j < 4; ++j) kv[j] = g * 256 + ((i ^ (4 * j + g)) * 16);
#pragma unroll
  for (int j = 0; j < 2; ++j) vv[j] = g * 256 + ((i ^ (8 * j + 2 * g)) * 16);
  const unsigned qv = w * 1024 + g * 256 + ((i ^ (4 * w + g)) * 16);
  const unsigned qso = (unsigned)bh * (16 * D * 2), qm0 = lds0 + LDS_Q + w * 1024, vm0 = ring + SLOT;

  asm volatile("" ::"v"(kv[0]), "v"(kv[1]), "v"(kv[2]), "v"(kv[3]), "v"(vv[0]), "v"(vv[1]), "v"(qv), "s"(qso), "s"(qm0), "s"(vm0), "s"(ring), "s"(ext0), "s"(ring0));
  asm volatile("" : "+s"(q), "+s"(k), "+s"(v));
  const auto krs = __builtin_amdgcn_make_buffer_rsrc((void*)k, 0, KV_BYTES, 0x00020000);
  const auto vrs = __builtin_amdgcn_make_buffer_rsrc((void*)v, 0, KV_BYTES, 0x00020000);
  const auto qrs = __builtin_amdgcn_make_buffer_rsrc((void*)q, 0, Q_BYTES, 0x00020000);

  auto issueK = [&](unsigned o) {
    dma4(ring, krs, o, kv[0], kv[1], kv[2], kv[3]);
    dma4(ring + 4096, krs, o + 4096, kv[0], kv[1], kv[2], kv[3]);
  };
  auto issueV = [&](unsigned o) {
    dma4(ring + SLOT, vrs, o, vv[0], vv[1], vv[0], vv[1]);
    dma4(ring + SLOT + 4096, vrs, o + 4096, vv[0], vv[1], vv[0], vv[1]);
  };
  auto tile_off = [&](int r) -> unsigned { return ring0 + (unsigned)(NW * r + ((w + r) & (NW - 1))) * (TILE * 256); };

  dma1(qm0, qrs, qso, qv);
  dma4(ring, krs, ext0, kv[0], kv[1], kv[2], kv[3]);
  dma4(vm0, vrs, ext0, vv[0], vv[1], vv[0], vv[1]);
  asm volatile("" : "+s"(ws), "+s"(out));
  const auto prs = __builtin_amdgcn_make_buffer_rsrc((void*)(ws + (size_t)bh * WS_PAIR), 0, WS_PAYLOAD, 0x00020000);

  LAS char* const ringp = (LAS char*)smem + w * RING;
  unsigned ka[4];
#pragma unroll
  for (int s = 0; s < 4; ++s) ka[s] = i * 256 + (((4 * s) ^ g ^ i) * 16);
  const int q_ = i >> 2, p_ = i & 3;
  const unsigned va0 = (4 * g + q_) * 256 + (((8 * (g & 1) + 2 * q_) + (p_ >> 1)) * 16) + (p_ & 1) * 8;

  wait_vm<8>();
  __builtin_amdgcn_s_barrier();
  asm volatile("" ::: "memory");
  u32x4 Qf[4];
#pragma unroll
  for (int s = 0; s < 4; ++s) Qf[s] = *(LAS u32x4*)((LAS char*)smem + LDS_Q + ka[s]);

  f32x4 acc[8];
  float m, l;
  unsigned cur = tile_off(0);

  {
    u32x4 kf[4];
    wait_vm<4>();
#pragma unroll
    for (int s = 0; s < 4; ++s) kf[s] = *(LAS u32x4*)(ringp + ka[s]);
    wait_lgkm();
    __builtin_amdgcn_sched_barrier(0);
    issueK(cur);
    __builtin_amdgcn_sched_barrier(0);
    f32x4 s0 = {0.f, 0.f, 0.f, 0.f};
#pragma unroll
    for (int s = 0; s < 4; ++s) s0 = __builtin_amdgcn_mfma_f32_16x16x32_bf16(asbf(kf[s]), asbf(Qf[s]), s0, 0, 0, 0);
    m = xmax(max4(s0));
    const float mc = -m * CEXP;
    float p[4];
#pragma unroll
    for (int r = 0; r < 4; ++r) p[r] = __builtin_amdgcn_exp2f(fmaf(s0[r], CEXP, mc));
    l = (p[0] + p[1]) + (p[2] + p[3]);
    const u32x2 P = {cvtpk(p[0], p[1]), cvtpk(p[2], p[3])};
    __builtin_amdgcn_sched_barrier(0);
    wait_vm<8>();
    __builtin_amdgcn_sched_barrier(0);
    s16x4 vf[8];
#pragma unroll
    for (int cb = 0; cb < 8; ++cb) vf[cb] = __builtin_amdgcn_ds_read_tr16_b64_v4i16((LAS s16x4*)(ringp + SLOT + (va0 ^ (cb << 5))));
    wait_lgkm();
    __builtin_amdgcn_sched_barrier(0);
#pragma unroll
    for (int cb = 0; cb < 8; ++cb)
      acc[cb] = __builtin_amdgcn_mfma_f32_16x16x16bf16_1k(vf[cb], __builtin_bit_cast(s16x4, P), f32x4{0.f, 0.f, 0.f, 0.f}, 0, 0, 0);
  }

  auto scores = [&](f32x4& s0, f32x4& s1) {
    u32x4 kf[8];
#pragma unroll
    for (int s = 0; s < 4; ++s) {
      kf[2 * s] = *(LAS u32x4*)(ringp + ka[s]);
      kf[2 * s + 1] = *(LAS u32x4*)(ringp + 4096 + ka[s]);
    }
    wait_lgkm();
    __builtin_amdgcn_sched_barrier(0);
    s0 = f32x4{0.f, 0.f, 0.f, 0.f};
    s1 = s0;
#pragma unroll
    for (int s = 0; s < 4; ++s) {
      s0 = __builtin_amdgcn_mfma_f32_16x16x32_bf16(asbf(kf[2 * s]), asbf(Qf[s]), s0, 0, 0, 0);
      s1 = __builtin_amdgcn_mfma_f32_16x16x32_bf16(asbf(kf[2 * s + 1]), asbf(Qf[s]), s1, 0, 0, 0);
    }
  };
  auto probs = [&](f32x4 s0, f32x4 s1) {
    const float mc = -m * CEXP;
    float p[8];
#pragma unroll
    for (int r = 0; r < 4; ++r) {
      p[r] = __builtin_amdgcn_exp2f(fmaf(s0[r], CEXP, mc));
      p[4 + r] = __builtin_amdgcn_exp2f(fmaf(s1[r], CEXP, mc));
    }
    l += ((p[0] + p[1]) + (p[2] + p[3])) + ((p[4] + p[5]) + (p[6] + p[7]));
    return u32x4{cvtpk(p[0], p[1]), cvtpk(p[2], p[3]), cvtpk(p[4], p[5]), cvtpk(p[6], p[7])};
  };
  auto rescale = [&](float mn) {
    const float alpha = __builtin_amdgcn_exp2f((m - mn) * CEXP);
    l *= alpha;
#pragma unroll
    for (int e = 0; e < 8; ++e) acc[e] *= alpha;
    m = mn;
  };
  auto values = [&](u32x4 P, bool drain) {
    s16x4 vf[16];
#pragma unroll
    for (int cb = 0; cb < 8; ++cb) {
      const unsigned a = va0 ^ (cb << 5);
      vf[2 * cb] = __builtin_amdgcn_ds_read_tr16_b64_v4i16((LAS s16x4*)(ringp + SLOT + a));
      vf[2 * cb + 1] = __builtin_amdgcn_ds_read_tr16_b64_v4i16((LAS s16x4*)(ringp + SLOT + 4096 + a));
    }
    if (drain) wait_lgkm();
    __builtin_amdgcn_sched_barrier(0);
#pragma unroll
    for (int cb = 0; cb < 8; ++cb) {
      const s16x8 f = __builtin_shufflevector(vf[2 * cb], vf[2 * cb + 1], 0, 1, 2, 3, 4, 5, 6, 7);
      acc[cb] = __builtin_amdgcn_mfma_f32_16x16x32_bf16(__builtin_bit_cast(bf16x8, f), asbf(P), acc[cb], 0, 0, 0);
    }
  };

#pragma unroll 1
  for (int r = 1; r < nr; ++r) {
    if (nr - r == CKPT && lane == 0) *(volatile LAS unsigned*)((LAS char*)smem + LDS_CW + 32 + w * 4) = 1u;
    wait_vm<AHEAD>();
    issueV(cur);
    wait_vm<8>();
    __builtin_amdgcn_sched_barrier(0);
    f32x4 s0, s1;
    scores(s0, s1);
    const float mn = fmaxf(m, xmax(fmaxf(max4(s0), max4(s1))));
    if (__builtin_amdgcn_ballot_w64(mn > m)) rescale(mn);
    const u32x4 P = probs(s0, s1);
    cur = tile_off(r);
    __builtin_amdgcn_sched_barrier(0);
    wait_vm<AHEAD>();
    issueK(cur);
    wait_vm<8>();
    __builtin_amdgcn_sched_barrier(0);
    values(P, true);
  }

  const int qq = tid >> 4, jj = tid & 15;
  LAS char* const sm = (LAS char*)smem;
  u32x4 ra0 = {0u, 0u, 0u, 0u}, ra1 = ra0, rst = ra0;
  f32x4 ta0 = {0.f, 0.f, 0.f, 0.f}, ta1 = ta0;
  float fb = 0.f, tl = 0.f;
  bool have = false;
  auto readRole = [&]() { return __builtin_amdgcn_readfirstlane(*(volatile LAS unsigned*)(sm + LDS_CW + 8)); };
  auto fetchLds = [&]() {
    const unsigned o0 = tid * 16, o1 = 4096 + tid * 16, o2 = WS_STATS + qq * 16, mb = lds0 + LDS_E + w * 3072;
    asm volatile(
        "s_mov_b32 m0, %0\n\t"
        "s_nop 0\n\t"
        "buffer_load_dwordx4 %4, %3, 0 offen sc1 lds\n\t"
        "s_mov_b32 m0, %1\n\t"
        "s_nop 0\n\t"
        "buffer_load_dwordx4 %5, %3, 0 offen sc1 lds\n\t"
        "s_mov_b32 m0, %2\n\t"
        "s_nop 0\n\t"
        "buffer_load_dwordx4 %6, %3, 0 offen sc1 lds"
        :
        : "s"(mb), "s"(mb + 1024), "s"(mb + 2048), "s"(prs), "v"(o0), "v"(o1), "v"(o2)
        : "memory");
  };

  unsigned role = readRole();
  wait_vm<AHEAD>();
  const bool early = role == ROLE_COMBINE;
  if (early) fetchLds();
  issueV(cur);
  if (early) wait_vm<11>(); else wait_vm<8>();
  __builtin_amdgcn_sched_barrier(0);
  f32x4 s0, s1;
  scores(s0, s1);
  const float mn = fmaxf(m, xmax(fmaxf(max4(s0), max4(s1))));
  if (g == 0) *(LAS float*)(sm + LDS_SM + i * 16 + w * 4) = mn;
  wait_lgkm();
  __builtin_amdgcn_s_barrier();
  asm volatile("" ::: "memory");
  if (role == ROLE_UNKNOWN) role = readRole();
  const bool late = role == ROLE_COMBINE && !early;
  if (late) fetchLds();
  const f32x4 mw = *(LAS f32x4*)(sm + LDS_SM + i * 16), mq = *(LAS f32x4*)(sm + LDS_SM + qq * 16);
  const u32x4 old0 = *(LAS u32x4*)(sm + LDS_OLD + tid * 16), old1 = *(LAS u32x4*)(sm + LDS_OLD + 4096 + tid * 16);
  const u32x4 olds = *(LAS u32x4*)(sm + LDS_OLD + WS_STATS + qq * 16);
  rescale(max4(mw));
#pragma unroll
  for (int e = 0; e < 8; ++e) asm volatile("" : "+v"(acc[e]));
  const u32x4 P = probs(s0, s1);
  l = xsum(l);
  if (g == 0) *(LAS float*)(sm + LDS_SL + i * 16 + w * 4) = l;
  const float M = max4(mq);

  auto arrived = [&]() {
    const unsigned ch = changed4(ra0, old0) & changed4(ra1, old1) & changed4(rst, olds);
    return __builtin_amdgcn_ballot_w64((ch & 1u) == 0) == 0;
  };
  auto prepare = [&]() {
#pragma clang fp contract(off)
    const float MA = __uint_as_float((rst[0] & 0xffff0000u) | (rst[1] >> 16));
    const float Mt = fmaxf(M, MA);
    fb = __builtin_amdgcn_exp2f((M - Mt) * CEXP);
    const float fa = __builtin_amdgcn_exp2f((MA - Mt) * CEXP);
    tl = __uint_as_float(rst[2] & ~1u) * fa;
    ta0 = clr0(ra0) * fa;
    ta1 = clr0(ra1) * fa;
  };
  auto takeFetched = [&]() {
    LAS char* e = sm + LDS_E + w * 3072 + lane * 16;
    ra0 = *(LAS u32x4*)e;
    ra1 = *(LAS u32x4*)(e + 1024);
    rst = *(LAS u32x4*)(e + 2048);
    have = arrived();
    if (have) prepare();
  };
  GAS char* dst = (GAS char*)out + (size_t)(bh * 16 + qq) * (D * 2) + jj * 16;
  const unsigned mb = __float_as_uint(M);
  u32x4 stw = flip4(u32x4{mb & 0xffff0000u, mb << 16, 0u, 0u}, olds);
  unsigned wr = ring + i * OROW + (g & 1) * 256 + (g >> 1) * 16, rd = lds0 + qq * OROW + jj * 16, rl = lds0 + LDS_SL + qq * 16;
  unsigned so0 = tid * 16, so1 = 4096 + tid * 16, so2 = WS_STATS + qq * 16;
  asm volatile("" : "+v"(stw), "+v"(dst), "+v"(wr), "+v"(rd), "+v"(rl), "+v"(so0), "+v"(so1), "+v"(so2));
  if (early) { wait_vm<8>(); takeFetched(); }
  __builtin_amdgcn_sched_barrier(0);
  if (late) wait_vm<3>(); else wait_vm<0>();
  __builtin_amdgcn_sched_barrier(0);
  values(P, false);
  {
    LAS char* d = (LAS char*)(size_t)wr;
#pragma unroll
    for (int cb = 0; cb < 8; ++cb) *(LAS f32x4*)(d + cb * 32) = acc[cb];
  }
  if (late) { wait_vm<0>(); takeFetched(); }
  wait_lgkm();
  __builtin_amdgcn_s_barrier();
  asm volatile("" ::: "memory");

  const f32x4 lw = *(LAS f32x4*)(size_t)rl;
  f32x4 pa[NW], pb[NW];
#pragma unroll
  for (int ww = 0; ww < NW; ++ww) {
    LAS char* src = (LAS char*)(size_t)rd + ww * RING;
    pa[ww] = *(LAS f32x4*)src;
    pb[ww] = *(LAS f32x4*)(src + 256);
  }
  const float L = (lw[0] + lw[1]) + (lw[2] + lw[3]);
  const f32x4 o0 = (pa[0] + pa[1]) + (pa[2] + pa[3]), o1 = (pb[0] + pb[1]) + (pb[2] + pb[3]);

  if (role != ROLE_COMBINE) {
    __builtin_amdgcn_raw_buffer_store_b128(flip4(asu(o0), old0), prs, so0, 0, 16);
    __builtin_amdgcn_raw_buffer_store_b128(flip4(asu(o1), old1), prs, so1, 0, 16);
    if (jj == 0) {
      stw[2] |= __float_as_uint(L) & ~1u;
      __builtin_amdgcn_raw_buffer_store_b128(stw, prs, so2, 0, 16);
    }
  } else {
    if (!have) {
      for (unsigned spin = 0; spin < SPIN_LIMIT; ++spin) {
        ra0 = __builtin_amdgcn_raw_buffer_load_b128(prs, so0, 0, 16);
        ra1 = __builtin_amdgcn_raw_buffer_load_b128(prs, so1, 0, 16);
        rst = __builtin_amdgcn_raw_buffer_load_b128(prs, so2, 0, 16);
        if (arrived()) break;
        __builtin_amdgcn_s_sleep(2);
      }
      prepare();
    }
    {
#pragma clang fp contract(off)
      const float inv = __builtin_amdgcn_rcpf(__uint_as_float(__float_as_uint(L) & ~1u) * fb + tl);
      const f32x4 r0 = (clr0(asu(o0)) * fb + ta0) * inv, r1 = (clr0(asu(o1)) * fb + ta1) * inv;
      const u32x4 o = {cvtpk(r0[0], r0[1]), cvtpk(r0[2], r0[3]), cvtpk(r1[0], r1[1]), cvtpk(r1[2], r1[3])};
      *(GAS u32x4*)dst = o;
    }
  }
  if (blockIdx.x == 0) {
    for (unsigned spin = 0; spin < (1u << 16); ++spin) {
      if (__builtin_amdgcn_s_memrealtime() >= tstart + 4000) break;
      __builtin_amdgcn_s_sleep(1);
    }
  }
}

extern "C" void kernel_launch(void* const* d_in, const int* in_sizes, int n_in, void* d_out, int out_size, void* d_ws,
                              size_t ws_size, hipStream_t stream) {
  (void)hipFuncSetAttribute((const void*)attn_fwd, hipFuncAttributeMaxDynamicSharedMemorySize, LDS_BYTES);
  attn_fwd<<<NBH * 2, NTHREADS, LDS_BYTES, stream>>>(d_in[0], d_in[1], d_in[2], d_out, (char*)d_ws);
}
```

```cpp
#include <hip/hip_runtime.h>
#include <hip/hip_bf16.h>
#include <stdint.h>
#include <type_traits>

typedef __attribute__((ext_vector_type(4))) unsigned u32x4;
typedef __attribute__((ext_vector_type(2))) unsigned u32x2;
typedef __attribute__((ext_vector_type(4))) float f32x4;
typedef __attribute__((ext_vector_type(4))) short s16x4;
typedef __attribute__((ext_vector_type(8))) short s16x8;
typedef __attribute__((ext_vector_type(8))) __bf16 bf16x8;
#define LAS __attribute__((address_space(3)))
#define GAS __attribute__((address_space(1)))

constexpr int B = 16, HQ = 32, HKV = 8, SQ = 4, SK = 2048, D = 128;
constexpr int NBH = B * HKV;
constexpr int NW = 4;
constexpr int TILE = 32;
constexpr int NTILES = SK / TILE;
constexpr float CEXP = 0.08838834764831845f * 1.4426950408889634f;
constexpr int SLOT = 8192, RING = 2 * SLOT;
constexpr int LDS_Q = NW * RING;
constexpr int LDS_SM = LDS_Q + 4096;
constexpr int LDS_SL = LDS_SM + 256;
constexpr int LDS_CW = LDS_SL + 256;
constexpr int LDS_OLD = LDS_CW + 256;
constexpr int LDS_E = LDS_OLD + 9 * 1024;
constexpr int LDS_E2 = LDS_E + NW * 3072 + 512;
constexpr int LDS_BYTES = 104 * 1024;
constexpr int OROW = 528;
constexpr unsigned SPIN_LIMIT = 1u << 20;
constexpr int WS_STATS = 8192;
constexpr int WS_PAYLOAD = WS_STATS + 256;
constexpr int WS_CTL = WS_PAYLOAD;
constexpr int CTL_HELLO = 0, CTL_JOIN = 2, CTL_ARR = 4;
constexpr int WS_PAIR = WS_CTL + 5 * 128;
constexpr unsigned ROLE_COMBINE = 0, ROLE_PUBLISH = 1, ROLE_UNKNOWN = 2;
constexpr int AHEAD = 4;
constexpr int CKPT = 1;
constexpr int POLL0 = 32, POLL1 = 16;
constexpr unsigned T_FIRST = 55, T_LAST_7 = 1690, T_LAST_8 = 1825, T_SPREAD = 100;
static_assert((HQ / HKV) * SQ == 16, "16 packed query rows per kv head");
static_assert(LDS_E2 + NW * 3072 <= LDS_BYTES && 16 * OROW <= RING, "LDS layout");

__device__ __forceinline__ unsigned cvtpk(float lo, float hi) {
  unsigned r;
  asm("v_cvt_pk_bf16_f32 %0, %1, %2" : "=v"(r) : "v"(lo), "v"(hi));
  return r;
}
__device__ __forceinline__ float xmax(float x) {
  auto s = __builtin_amdgcn_permlane16_swap(__float_as_uint(x), __float_as_uint(x), false, false);
  x = fmaxf(__uint_as_float(s[0]), __uint_as_float(s[1]));
  auto t = __builtin_amdgcn_permlane32_swap(__float_as_uint(x), __float_as_uint(x), false, false);
  return fmaxf(__uint_as_float(t[0]), __uint_as_float(t[1]));
}
__device__ __forceinline__ float xsum(float x) {
  auto s = __builtin_amdgcn_permlane16_swap(__float_as_uint(x), __float_as_uint(x), false, false);
  x = __uint_as_float(s[0]) + __uint_as_float(s[1]);
  auto t = __builtin_amdgcn_permlane32_swap(__float_as_uint(x), __float_as_uint(x), false, false);
  return __uint_as_float(t[0]) + __uint_as_float(t[1]);
}
__device__ __forceinline__ bf16x8 asbf(u32x4 v) { return __builtin_bit_cast(bf16x8, v); }

__device__ __forceinline__ void dma4(unsigned m0v, __amdgpu_buffer_rsrc_t srd, unsigned soff, unsigned v0, unsigned v1,
                                     unsigned v2, unsigned v3) {
  asm volatile(
      "s_mov_b32 m0, %0\n\t"
      "s_nop 0\n\t"
      "buffer_load_dwordx4 %3, %1, %2 offen nt lds\n\t"
      "buffer_load_dwordx4 %4, %1, %2 offen offset:1024 nt lds\n\t"
      "buffer_load_dwordx4 %5, %1, %2 offen offset:2048 nt lds\n\t"
      "buffer_load_dwordx4 %6, %1, %2 offen offset:3072 nt lds"
      :
      : "s"(m0v), "s"(srd), "s"(soff), "v"(v0), "v"(v1), "v"(v2), "v"(v3)
      : "memory");
}
__device__ __forceinline__ void dma1(unsigned m0v, __amdgpu_buffer_rsrc_t srd, unsigned soff, unsigned v0) {
  asm volatile(
      "s_mov_b32 m0, %0\n\t"
      "s_nop 0\n\t"
      "buffer_load_dwordx4 %3, %1, %2 offen nt lds"
      :
      : "s"(m0v), "s"(srd), "s"(soff), "v"(v0)
      : "memory");
}
template <int N>
__device__ __forceinline__ void wait_vm() { asm volatile("s_waitcnt vmcnt(%0)" ::"i"(N) : "memory"); }
__device__ __forceinline__ void wait_lgkm() { asm volatile("s_waitcnt lgkmcnt(0)" ::: "memory"); }

__device__ __forceinline__ unsigned ld_agent(const unsigned* p) { return __hip_atomic_load((GAS const unsigned*)p, __ATOMIC_RELAXED, __HIP_MEMORY_SCOPE_AGENT); }
__device__ __forceinline__ void st_agent(unsigned* p, unsigned v) { __hip_atomic_store((GAS unsigned*)p, v, __ATOMIC_RELAXED, __HIP_MEMORY_SCOPE_AGENT); }
__device__ __forceinline__ u32x4 asu(f32x4 x) { return __builtin_bit_cast(u32x4, x); }
__device__ __forceinline__ u32x4 flip4(u32x4 u, u32x4 old) {
  return u32x4{(u[0] & ~1u) | (~old[0] & 1u), (u[1] & ~1u) | (~old[1] & 1u), (u[2] & ~1u) | (~old[2] & 1u), (u[3] & ~1u) | (~old[3] & 1u)};
}
__device__ __forceinline__ unsigned changed4(u32x4 a, u32x4 old) { return ((a[0] ^ old[0]) & (a[1] ^ old[1])) & ((a[2] ^ old[2]) & (a[3] ^ old[3])); }

__device__ __forceinline__ f32x4 clr0(u32x4 u) { return __builtin_bit_cast(f32x4, u & ~1u); }
__device__ __forceinline__ float max4(f32x4 x) { return fmaxf(fmaxf(x[0], x[1]), fmaxf(x[2], x[3])); }

constexpr int NTHREADS = (NW + 1) * 64;
constexpr int NX = 16;
constexpr unsigned KV_BYTES = (unsigned)B * HKV * SK * D * 2, Q_BYTES = (unsigned)B * HQ * SQ * D * 2;

__global__ __launch_bounds__(NTHREADS) void attn_fwd(const void* __restrict__ q, const void* __restrict__ k, const void* __restrict__ v,
                                                     void* __restrict__ out, char* __restrict__ ws
) {
  extern __shared__ __attribute__((aligned(16))) char smem[];
  const bool lng = !(blockIdx.x & 1);
  const int tid = threadIdx.x, lane = tid & 63;
  const int w = __builtin_amdgcn_readfirstlane(tid >> 6);
  const int x8 = blockIdx.x & 7;
  const bool isP = x8 < 4;
  const int bh = (blockIdx.x >> 3) * 4 + (isP ? x8 : (x8 ^ 5));
  const int i = lane & 15, g = lane >> 4;
  const unsigned lds0 = (unsigned)(size_t)(LAS char*)smem;

  if (w == NW) {
    asm volatile("" ::"s"(q), "s"(k), "s"(v), "s"(out));
    char* const pair = ws + (size_t)bh * WS_PAIR;
    const auto prs = __builtin_amdgcn_make_buffer_rsrc((void*)pair, 0, WS_PAYLOAD, 0x00020000);
    LAS unsigned* const cw = (LAS unsigned*)((LAS char*)smem + LDS_CW);
    unsigned* const ctl = (unsigned*)(pair + WS_CTL);
    const int me = isP ? 1 : 0;
    if (lane < 12) cw[lane] = lane == 2 ? ROLE_UNKNOWN : 0u;
    unsigned cv;
    {
      const unsigned vo = lane * 16, vo4 = 4096 + lane * 16, vo8 = 8192 + lane * 16;
      asm volatile(
          "global_load_dword %0, %1, off sc1\n\t"
          "s_mov_b32 m0, %2\n\t"
          "s_nop 0\n\t"
          "buffer_load_dwordx4 %6, %5, 0 offen sc1 nt lds\n\t"
          "buffer_load_dwordx4 %6, %5, 0 offen offset:1024 sc1 nt lds\n\t"
          "buffer_load_dwordx4 %6, %5, 0 offen offset:2048 sc1 nt lds\n\t"
          "buffer_load_dwordx4 %6, %5, 0 offen offset:3072 sc1 nt lds\n\t"
          "s_mov_b32 m0, %3\n\t"
          "s_nop 0\n\t"
          "buffer_load_dwordx4 %7, %5, 0 offen sc1 nt lds\n\t"
          "buffer_load_dwordx4 %7, %5, 0 offen offset:1024 sc1 nt lds\n\t"
          "buffer_load_dwordx4 %7, %5, 0 offen offset:2048 sc1 nt lds\n\t"
          "buffer_load_dwordx4 %7, %5, 0 offen offset:3072 sc1 nt lds\n\t"
          "s_mov_b32 m0, %4\n\t"
          "s_nop 0\n\t"
          "buffer_load_dwordx4 %8, %5, 0 offen sc1 nt lds\n\t"
          "s_waitcnt lgkmcnt(0)\n\t"
          "s_barrier\n\t"
          "s_waitcnt vmcnt(0)"
          : "=&v"(cv)
          : "v"(ctl + min(lane, 4) * 32), "s"(lds0 + LDS_OLD), "s"(lds0 + LDS_OLD + 4096), "s"(lds0 + LDS_OLD + 8192), "s"(prs), "v"(vo), "v"(vo4), "v"(vo8)
          : "memory");
    }
    const unsigned hello_mine0 = __builtin_amdgcn_readlane(cv, CTL_HELLO + me), hello_peer0 = __builtin_amdgcn_readlane(cv, CTL_HELLO + 1 - me);
    const unsigned join_peer0 = __builtin_amdgcn_readlane(cv, CTL_JOIN + 1 - me), arr0 = __builtin_amdgcn_readlane(cv, CTL_ARR);
    unsigned hello = hello_mine0 + 1;
    if (hello == join_peer0) ++hello;
    if (lane == 0) st_agent(ctl + (CTL_HELLO + me) * 32, hello);
    bool dyn = false;
    const unsigned* const pollp = ctl + ((lane & 1) ? CTL_JOIN + 1 - me : CTL_HELLO + 1 - me) * 32;
    __builtin_amdgcn_s_sleep(POLL0);
    for (unsigned spin = 0; spin < SPIN_LIMIT; ++spin) {
      if (!dyn) {
        unsigned x = 0;
        if (lane < 2) x = ld_agent(pollp);
        const unsigned xh = __builtin_amdgcn_readlane(x, 0), xj = __builtin_amdgcn_readlane(x, 1);
        if (xh != hello_peer0) {
          dyn = true;
          if (lane == 0) st_agent(ctl + (CTL_JOIN + me) * 32, xh);
        } else if (xj == hello) {
          dyn = true;
        }
        if (!dyn) { __builtin_amdgcn_s_sleep(POLL1); continue; }
      }
      const u32x4 f = *(volatile LAS u32x4*)(cw + 8);
      if ((f[0] & f[1] & f[2] & f[3]) != 0) break;
      __builtin_amdgcn_s_sleep(4);
    }
    unsigned old = 0;
    if (lane == 0) old = __hip_atomic_fetch_add((GAS unsigned*)(ctl + CTL_ARR * 32), 1u, __ATOMIC_RELAXED, __HIP_MEMORY_SCOPE_AGENT);
    const unsigned role = __builtin_amdgcn_readfirstlane(old) == arr0 ? ROLE_PUBLISH : ROLE_COMBINE;
    if (lane == 0) *(volatile LAS unsigned*)(cw + 2) = role;
    wait_lgkm();
    return;
  }

  const int nr = lng ? 8 : 7;
  const unsigned bhoff = (unsigned)bh * (SK * D * 2);
  const unsigned ring0 = bhoff + (isP ? 0 : SK - nr * NW * TILE) * (D * 2);
  const unsigned ext0 = bhoff + ((isP ? nr * NW * TILE : SK - nr * NW * TILE - NW * NX) + w * NX) * (D * 2);
  const unsigned ring = lds0 + w * RING;

  unsigned kv[4], vv[2];
#pragma unroll
  for (int j = 0; j < 4; ++j) kv[j] = g * 256 + ((i ^ (4 * j + g)) * 16);
#pragma unroll
  for (int j = 0; j < 2; ++j) vv[j] = g * 256 + ((i ^ (8 * j + 2 * g)) * 16);
  const unsigned qv = w * 1024 + g * 256 + ((i ^ (4 * w + g)) * 16);
  const unsigned qso = (unsigned)bh * (16 * D * 2), qm0 = lds0 + LDS_Q + w * 1024, vm0 = ring + SLOT;

  asm volatile("" ::"v"(kv[0]), "v"(kv[1]), "v"(kv[2]), "v"(kv[3]), "v"(vv[0]), "v"(vv[1]), "v"(qv), "s"(qso), "s"(qm0), "s"(vm0), "s"(ring), "s"(ext0), "s"(ring0));
  asm volatile("" : "+s"(q), "+s"(k), "+s"(v));
  const auto krs = __builtin_amdgcn_make_buffer_rsrc((void*)k, 0, KV_BYTES, 0x00020000);
  const auto vrs = __builtin_amdgcn_make_buffer_rsrc((void*)v, 0, KV_BYTES, 0x00020000);
  const auto qrs = __builtin_amdgcn_make_buffer_rsrc((void*)q, 0, Q_BYTES, 0x00020000);

  auto issueK = [&](unsigned o) {
    dma4(ring, krs, o, kv[0], kv[1], kv[2], kv[3]);
    dma4(ring + 4096, krs, o + 4096, kv[0], kv[1], kv[2], kv[3]);
  };
  auto issueV = [&](unsigned o) {
    dma4(ring + SLOT, vrs, o, vv[0], vv[1], vv[0], vv[1]);
    dma4(ring + SLOT + 4096, vrs, o + 4096, vv[0], vv[1], vv[0], vv[1]);
  };
  auto tile_off = [&](int r) -> unsigned { return ring0 + (unsigned)(NW * r + ((w + r) & (NW - 1))) * (TILE * 256); };

  dma1(qm0, qrs, qso, qv);
  dma4(ring, krs, ext0, kv[0], kv[1], kv[2], kv[3]);
  dma4(vm0, vrs, ext0, vv[0], vv[1], vv[0], vv[1]);
  asm volatile("" : "+s"(ws), "+s"(out));
  const unsigned tau = lng ? (T_LAST_8 - T_FIRST) * 256 / 16 : (T_LAST_7 - T_FIRST) * 256 / 14;
  const unsigned tbase = (unsigned)__builtin_amdgcn_s_memrealtime() + T_FIRST - ((unsigned)(bh * T_SPREAD) >> 7);
  auto now = [&]() -> unsigned { return (unsigned)__builtin_amdgcn_s_memrealtime(); };
  auto pace = [&](int u, unsigned t) {
    const unsigned due = tbase + ((u * tau) >> 8);
#pragma unroll 1
    for (int z = 0; z < 4096 && (int)(t - due) < 0; ++z) {
      __builtin_amdgcn_s_sleep(1);
      t = (unsigned)__builtin_amdgcn_s_memrealtime();
    }
  };
  const auto prs = __builtin_amdgcn_make_buffer_rsrc((void*)(ws + (size_t)bh * WS_PAIR), 0, WS_PAYLOAD, 0x00020000);

  LAS char* const ringp = (LAS char*)smem + w * RING;
  unsigned ka[4];
#pragma unroll
  for (int s = 0; s < 4; ++s) ka[s] = i * 256 + (((4 * s) ^ g ^ i) * 16);
  const int q_ = i >> 2, p_ = i & 3;
  const unsigned va0 = (4 * g + q_) * 256 + (((8 * (g & 1) + 2 * q_) + (p_ >> 1)) * 16) + (p_ & 1) * 8;

  wait_vm<8>();
  __builtin_amdgcn_s_barrier();
  asm volatile("" ::: "memory");
  u32x4 Qf[4];
#pragma unroll
  for (int s = 0; s < 4; ++s) Qf[s] = *(LAS u32x4*)((LAS char*)smem + LDS_Q + ka[s]);

  f32x4 acc[8];
  float m, l;
  unsigned cur = tile_off(0);

  {
    u32x4 kf[4];
    wait_vm<4>();
#pragma unroll
    for (int s = 0; s < 4; ++s) kf[s] = *(LAS u32x4*)(ringp + ka[s]);
    wait_lgkm();
    __builtin_amdgcn_sched_barrier(0);
    issueK(cur);
    __builtin_amdgcn_sched_barrier(0);
    f32x4 s0 = {0.f, 0.f, 0.f, 0.f};
#pragma unroll
    for (int s = 0; s < 4; ++s) s0 = __builtin_amdgcn_mfma_f32_16x16x32_bf16(asbf(kf[s]), asbf(Qf[s]), s0, 0, 0, 0);
    m = xmax(max4(s0));
    const float mc = -m * CEXP;
    float p[4];
#pragma unroll
    for (int r = 0; r < 4; ++r) p[r] = __builtin_amdgcn_exp2f(fmaf(s0[r], CEXP, mc));
    l = (p[0] + p[1]) + (p[2] + p[3]);
    const u32x2 P = {cvtpk(p[0], p[1]), cvtpk(p[2], p[3])};
    __builtin_amdgcn_sched_barrier(0);
    wait_vm<8>();
    __builtin_amdgcn_sched_barrier(0);
    s16x4 vf[8];
#pragma unroll
    for (int cb = 0; cb < 8; ++cb) vf[cb] = __builtin_amdgcn_ds_read_tr16_b64_v4i16((LAS s16x4*)(ringp + SLOT + (va0 ^ (cb << 5))));
    wait_lgkm();
    __builtin_amdgcn_sched_barrier(0);
#pragma unroll
    for (int cb = 0; cb < 8; ++cb)
      acc[cb] = __builtin_amdgcn_mfma_f32_16x16x16bf16_1k(vf[cb], __builtin_bit_cast(s16x4, P), f32x4{0.f, 0.f, 0.f, 0.f}, 0, 0, 0);
  }

  auto scores = [&](f32x4& s0, f32x4& s1) {
    u32x4 kf[8];
#pragma unroll
    for (int s = 0; s < 4; ++s) {
      kf[2 * s] = *(LAS u32x4*)(ringp + ka[s]);
      kf[2 * s + 1] = *(LAS u32x4*)(ringp + 4096 + ka[s]);
    }
    wait_lgkm();
    __builtin_amdgcn_sched_barrier(0);
    s0 = f32x4{0.f, 0.f, 0.f, 0.f};
    s1 = s0;
#pragma unroll
    for (int s = 0; s < 4; ++s) {
      s0 = __builtin_amdgcn_mfma_f32_16x16x32_bf16(asbf(kf[2 * s]), asbf(Qf[s]), s0, 0, 0, 0);
      s1 = __builtin_amdgcn_mfma_f32_16x16x32_bf16(asbf(kf[2 * s + 1]), asbf(Qf[s]), s1, 0, 0, 0);
    }
  };
  auto probs = [&](f32x4 s0, f32x4 s1) {
    const float mc = -m * CEXP;
    float p[8];
#pragma unroll
    for (int r = 0; r < 4; ++r) {
      p[r] = __builtin_amdgcn_exp2f(fmaf(s0[r], CEXP, mc));
      p[4 + r] = __builtin_amdgcn_exp2f(fmaf(s1[r], CEXP, mc));
    }
    l += ((p[0] + p[1]) + (p[2] + p[3])) + ((p[4] + p[5]) + (p[6] + p[7]));
    return u32x4{cvtpk(p[0], p[1]), cvtpk(p[2], p[3]), cvtpk(p[4], p[5]), cvtpk(p[6], p[7])};
  };
  auto rescale = [&](float mn) {
    const float alpha = __builtin_amdgcn_exp2f((m - mn) * CEXP);
    l *= alpha;
#pragma unroll
    for (int e = 0; e < 8; ++e) acc[e] *= alpha;
    m = mn;
  };
  auto values = [&](u32x4 P, bool drain) {
    s16x4 vf[16];
#pragma unroll
    for (int cb = 0; cb < 8; ++cb) {
      const unsigned a = va0 ^ (cb << 5);
      vf[2 * cb] = __builtin_amdgcn_ds_read_tr16_b64_v4i16((LAS s16x4*)(ringp + SLOT + a));
      vf[2 * cb + 1] = __builtin_amdgcn_ds_read_tr16_b64_v4i16((LAS s16x4*)(ringp + SLOT + 4096 + a));
    }
    if (drain) wait_lgkm();
    __builtin_amdgcn_sched_barrier(0);
#pragma unroll
    for (int cb = 0; cb < 8; ++cb) {
      const s16x8 f = __builtin_shufflevector(vf[2 * cb], vf[2 * cb + 1], 0, 1, 2, 3, 4, 5, 6, 7);
      acc[cb] = __builtin_amdgcn_mfma_f32_16x16x32_bf16(__builtin_bit_cast(bf16x8, f), asbf(P), acc[cb], 0, 0, 0);
    }
  };

#pragma unroll 1
  for (int r = 1; r < nr; ++r) {
    if (nr - r == CKPT && lane == 0) *(volatile LAS unsigned*)((LAS char*)smem + LDS_CW + 32 + w * 4) = 1u;
    { const unsigned t = now(); wait_vm<AHEAD>(); pace(2 * r, t); }
    issueV(cur);
    wait_vm<8>();
    __builtin_amdgcn_sched_barrier(0);
    f32x4 s0, s1;
    scores(s0, s1);
    const float mn = fmaxf(m, xmax(fmaxf(max4(s0), max4(s1))));
    if (__builtin_amdgcn_ballot_w64(mn > m)) rescale(mn);
    const u32x4 P = probs(s0, s1);
    cur = tile_off(r);
    __builtin_amdgcn_sched_barrier(0);
    { const unsigned t = now(); wait_vm<AHEAD>(); pace(2 * r + 1, t); }
    issueK(cur);
    wait_vm<8>();
    __builtin_amdgcn_sched_barrier(0);
    values(P, true);
  }

  const int qq = tid >> 4, jj = tid & 15;
  LAS char* const sm = (LAS char*)smem;
  u32x4 ra0 = {0u, 0u, 0u, 0u}, ra1 = ra0, rst = ra0;
  f32x4 ta0 = {0.f, 0.f, 0.f, 0.f}, ta1 = ta0;
  float fb = 0.f, tl = 0.f;
  bool have = false;
  auto readRole = [&]() { return __builtin_amdgcn_readfirstlane(*(volatile LAS unsigned*)(sm + LDS_CW + 8)); };
  auto fetchLds = [&](int area) {
    const unsigned o0 = tid * 16, o1 = 4096 + tid * 16, o2 = WS_STATS + qq * 16, mb = lds0 + area + w * 3072;
    asm volatile(
        "s_mov_b32 m0, %0\n\t"
        "s_nop 0\n\t"
        "buffer_load_dwordx4 %4, %3, 0 offen sc1 lds\n\t"
        "s_mov_b32 m0, %1\n\t"
        "s_nop 0\n\t"
        "buffer_load_dwordx4 %5, %3, 0 offen sc1 lds\n\t"
        "s_mov_b32 m0, %2\n\t"
        "s_nop 0\n\t"
        "buffer_load_dwordx4 %6, %3, 0 offen sc1 lds"
        :
        : "s"(mb), "s"(mb + 1024), "s"(mb + 2048), "s"(prs), "v"(o0), "v"(o1), "v"(o2)
        : "memory");
  };

  unsigned role = readRole();
  { const unsigned t = now(); wait_vm<AHEAD>(); pace(2 * nr, t); }
  issueV(cur);
  wait_vm<8>();
  __builtin_amdgcn_sched_barrier(0);
  f32x4 s0, s1;
  scores(s0, s1);
  const float mn = fmaxf(m, xmax(fmaxf(max4(s0), max4(s1))));
  if (g == 0) *(LAS float*)(sm + LDS_SM + i * 16 + w * 4) = mn;
  wait_lgkm();
  __builtin_amdgcn_s_barrier();
  asm volatile("" ::: "memory");
  if (role == ROLE_UNKNOWN) role = readRole();
  const bool late = role == ROLE_COMBINE;
  if (late) fetchLds(LDS_E2);
  const f32x4 mw = *(LAS f32x4*)(sm + LDS_SM + i * 16), mq = *(LAS f32x4*)(sm + LDS_SM + qq * 16);
  const u32x4 old0 = *(LAS u32x4*)(sm + LDS_OLD + tid * 16), old1 = *(LAS u32x4*)(sm + LDS_OLD + 4096 + tid * 16);
  const u32x4 olds = *(LAS u32x4*)(sm + LDS_OLD + WS_STATS + qq * 16);
  rescale(max4(mw));
#pragma unroll
  for (int e = 0; e < 8; ++e) asm volatile("" : "+v"(acc[e]));
  const u32x4 P = probs(s0, s1);
  l = xsum(l);
  if (g == 0) *(LAS float*)(sm + LDS_SL + i * 16 + w * 4) = l;
  const float M = max4(mq);

  auto arrived = [&]() {
    const unsigned ch = changed4(ra0, old0) & changed4(ra1, old1) & changed4(rst, olds);
    return __builtin_amdgcn_ballot_w64((ch & 1u) == 0) == 0;
  };
  auto prepare = [&]() {
#pragma clang fp contract(off)
    const float MA = __uint_as_float((rst[0] & 0xffff0000u) | (rst[1] >> 16));
    const float Mt = fmaxf(M, MA);
    fb = __builtin_amdgcn_exp2f((M - Mt) * CEXP);
    const float fa = __builtin_amdgcn_exp2f((MA - Mt) * CEXP);
    tl = __uint_as_float(rst[2] & ~1u) * fa;
    ta0 = clr0(ra0) * fa;
    ta1 = clr0(ra1) * fa;
  };
  auto takeFetched = [&](int area) {
    LAS char* e = sm + area + w * 3072 + lane * 16;
    ra0 = *(LAS u32x4*)e;
    ra1 = *(LAS u32x4*)(e + 1024);
    rst = *(LAS u32x4*)(e + 2048);
    have = arrived();
    if (have) prepare();
  };
  GAS char* dst = (GAS char*)out + (size_t)(bh * 16 + qq) * (D * 2) + jj * 16;
  const unsigned mb = __float_as_uint(M);
  u32x4 stw = flip4(u32x4{mb & 0xffff0000u, mb << 16, 0u, 0u}, olds);
  unsigned wr = ring + i * OROW + (g & 1) * 256 + (g >> 1) * 16, rd = lds0 + qq * OROW + jj * 16, rl = lds0 + LDS_SL + qq * 16;
  unsigned so0 = tid * 16, so1 = 4096 + tid * 16, so2 = WS_STATS + qq * 16;
  asm volatile("" : "+v"(stw), "+v"(dst), "+v"(wr), "+v"(rd), "+v"(rl), "+v"(so0), "+v"(so1), "+v"(so2));
  __builtin_amdgcn_sched_barrier(0);
  if (late) wait_vm<3>(); else wait_vm<0>();
  __builtin_amdgcn_sched_barrier(0);
  values(P, false);
  {
    LAS char* d = (LAS char*)(size_t)wr;
#pragma unroll
    for (int cb = 0; cb < 8; ++cb) *(LAS f32x4*)(d + cb * 32) = acc[cb];
  }
  if (late) { wait_vm<0>(); takeFetched(LDS_E2); }
  const bool again = late && !have;
  if (again) fetchLds(LDS_E);
  wait_lgkm();
  __builtin_amdgcn_s_barrier();
  asm volatile("" ::: "memory");

  const f32x4 lw = *(LAS f32x4*)(size_t)rl;
  f32x4 pa[NW], pb[NW];
#pragma unroll
  for (int ww = 0; ww < NW; ++ww) {
    LAS char* src = (LAS char*)(size_t)rd + ww * RING;
    pa[ww] = *(LAS f32x4*)src;
    pb[ww] = *(LAS f32x4*)(src + 256);
  }
  const float L = (lw[0] + lw[1]) + (lw[2] + lw[3]);
  const f32x4 o0 = (pa[0] + pa[1]) + (pa[2] + pa[3]), o1 = (pb[0] + pb[1]) + (pb[2] + pb[3]);

  if (role != ROLE_COMBINE) {
    __builtin_amdgcn_raw_buffer_store_b128(flip4(asu(o0), old0), prs, so0, 0, 16);
    __builtin_amdgcn_raw_buffer_store_b128(flip4(asu(o1), old1), prs, so1, 0, 16);
    if (jj == 0) {
      stw[2] |= __float_as_uint(L) & ~1u;
      __builtin_amdgcn_raw_buffer_store_b128(stw, prs, so2, 0, 16);
    }
  } else {
    wait_vm<0>();
    if (again) takeFetched(LDS_E);
    if (!have) {
      for (unsigned spin = 0; spin < SPIN_LIMIT; ++spin) {
        ra0 = __builtin_amdgcn_raw_buffer_load_b128(prs, so0, 0, 16);
        ra1 = __builtin_amdgcn_raw_buffer_load_b128(prs, so1, 0, 16);
        rst = __builtin_amdgcn_raw_buffer_load_b128(prs, so2, 0, 16);
        if (arrived()) break;
        __builtin_amdgcn_s_sleep(2);
      }
      prepare();
    }
    {
#pragma clang fp contract(off)
      const float inv = __builtin_amdgcn_rcpf(__uint_as_float(__float_as_uint(L) & ~1u) * fb + tl);
      const f32x4 r0 = (clr0(asu(o0)) * fb + ta0) * inv, r1 = (clr0(asu(o1)) * fb + ta1) * inv;
      const u32x4 o = {cvtpk(r0[0], r0[1]), cvtpk(r0[2], r0[3]), cvtpk(r1[0], r1[1]), cvtpk(r1[2], r1[3])};
      *(GAS u32x4*)dst = o;
    }
  }
}

extern "C" void kernel_launch(void* const* d_in, const int* in_sizes, int n_in, void* d_out, int out_size, void* d_ws,
                              size_t ws_size, hipStream_t stream) {
  (void)hipFuncSetAttribute((const void*)attn_fwd, hipFuncAttributeMaxDynamicSharedMemorySize, LDS_BYTES);
  attn_fwd<<<NBH * 2, NTHREADS, LDS_BYTES, stream>>>(d_in[0], d_in[1], d_in[2], d_out, (char*)d_ws);
}
```

```cpp
#include <hip/hip_runtime.h>
#include <hip/hip_bf16.h>
#include <stdint.h>
#include <type_traits>

typedef __attribute__((ext_vector_type(4))) unsigned u32x4;
typedef __attribute__((ext_vector_type(2))) unsigned u32x2;
typedef __attribute__((ext_vector_type(4))) float f32x4;
typedef __attribute__((ext_vector_type(4))) short s16x4;
typedef __attribute__((ext_vector_type(8))) short s16x8;
typedef __attribute__((ext_vector_type(8))) __bf16 bf16x8;
#define LAS __attribute__((address_space(3)))
#define GAS __attribute__((address_space(1)))

constexpr int B = 16, HQ = 32, HKV = 8, SQ = 4, SK = 2048, D = 128;
constexpr int NBH = B * HKV;
constexpr int NW = 4;
constexpr int TILE = 32;
constexpr int NTILES = SK / TILE;
constexpr float CEXP = 0.08838834764831845f * 1.4426950408889634f;
constexpr int SLOT = 8192, RING = 2 * SLOT;
constexpr int LDS_Q = NW * RING;
constexpr int LDS_SM = LDS_Q + 4096;
constexpr int LDS_SL = LDS_SM + 256;
constexpr int LDS_CW = LDS_SL + 256;
constexpr int LDS_OLD = LDS_CW + 256;
constexpr int LDS_E = LDS_OLD + 9 * 1024;
constexpr int LDS_E2 = LDS_E + NW * 3072 + 512;
constexpr int LDS_BYTES = 104 * 1024;
constexpr int OROW = 528;
constexpr unsigned SPIN_LIMIT = 1u << 20;
constexpr int WS_STATS = 8192;
constexpr int WS_PAYLOAD = WS_STATS + 256;
constexpr int WS_CTL = WS_PAYLOAD;
constexpr int CTL_HELLO = 0, CTL_JOIN = 2, CTL_ARR = 4;
constexpr int WS_PAIR = WS_CTL + 5 * 128;
constexpr unsigned ROLE_COMBINE = 0, ROLE_PUBLISH = 1, ROLE_UNKNOWN = 2;
constexpr int AHEAD = 4;
constexpr int CKPT = 1;
constexpr int POLL0 = 32, POLL1 = 16;
constexpr unsigned T_FIRST = 55, T_LAST_7 = 1690, T_LAST_8 = 1825, T_SPREAD = 100;
static_assert((HQ / HKV) * SQ == 16, "16 packed query rows per kv head");
static_assert(LDS_E2 + NW * 3072 <= LDS_BYTES && 16 * OROW <= RING, "LDS layout");

__device__ __forceinline__ unsigned cvtpk(float lo, float hi) {
  unsigned r;
  asm("v_cvt_pk_bf16_f32 %0, %1, %2" : "=v"(r) : "v"(lo), "v"(hi));
  return r;
}
__device__ __forceinline__ float xmax(float x) {
  auto s = __builtin_amdgcn_permlane16_swap(__float_as_uint(x), __float_as_uint(x), false, false);
  x = fmaxf(__uint_as_float(s[0]), __uint_as_float(s[1]));
  auto t = __builtin_amdgcn_permlane32_swap(__float_as_uint(x), __float_as_uint(x), false, false);
  return fmaxf(__uint_as_float(t[0]), __uint_as_float(t[1]));
}
__device__ __forceinline__ float xsum(float x) {
  auto s = __builtin_amdgcn_permlane16_swap(__float_as_uint(x), __float_as_uint(x), false, false);
  x = __uint_as_float(s[0]) + __uint_as_float(s[1]);
  auto t = __builtin_amdgcn_permlane32_swap(__float_as_uint(x), __float_as_uint(x), false, false);
  return __uint_as_float(t[0]) + __uint_as_float(t[1]);
}
__device__ __forceinline__ bf16x8 asbf(u32x4 v) { return __builtin_bit_cast(bf16x8, v); }

__device__ __forceinline__ void dma4(unsigned m0v, __amdgpu_buffer_rsrc_t srd, unsigned soff, unsigned v0, unsigned v1,
                                     unsigned v2, unsigned v3) {
  asm volatile(
      "s_mov_b32 m0, %0\n\t"
      "s_nop 0\n\t"
      "buffer_load_dwordx4 %3, %1, %2 offen nt lds\n\t"
      "buffer_load_dwordx4 %4, %1, %2 offen offset:1024 nt lds\n\t"
      "buffer_load_dwordx4 %5, %1, %2 offen offset:2048 nt lds\n\t"
      "buffer_load_dwordx4 %6, %1, %2 offen offset:3072 nt lds"
      :
      : "s"(m0v), "s"(srd), "s"(soff), "v"(v0), "v"(v1), "v"(v2), "v"(v3)
      : "memory");
}
__device__ __forceinline__ void dma1(unsigned m0v, __amdgpu_buffer_rsrc_t srd, unsigned soff, unsigned v0) {
  asm volatile(
      "s_mov_b32 m0, %0\n\t"
      "s_nop 0\n\t"
      "buffer_load_dwordx4 %3, %1, %2 offen nt lds"
      :
      : "s"(m0v), "s"(srd), "s"(soff), "v"(v0)
      : "memory");
}
template <int N>
__device__ __forceinline__ void wait_vm() { asm volatile("s_waitcnt vmcnt(%0)" ::"i"(N) : "memory"); }
__device__ __forceinline__ void wait_lgkm() { asm volatile("s_waitcnt lgkmcnt(0)" ::: "memory"); }

__device__ __forceinline__ unsigned ld_agent(const unsigned* p) { return __hip_atomic_load((GAS const unsigned*)p, __ATOMIC_RELAXED, __HIP_MEMORY_SCOPE_AGENT); }
__device__ __forceinline__ void st_agent(unsigned* p, unsigned v) { __hip_atomic_store((GAS unsigned*)p, v, __ATOMIC_RELAXED, __HIP_MEMORY_SCOPE_AGENT); }
__device__ __forceinline__ u32x4 asu(f32x4 x) { return __builtin_bit_cast(u32x4, x); }
__device__ __forceinline__ u32x4 flip4(u32x4 u, u32x4 old) {
  return u32x4{(u[0] & ~1u) | (~old[0] & 1u), (u[1] & ~1u) | (~old[1] & 1u), (u[2] & ~1u) | (~old[2] & 1u), (u[3] & ~1u) | (~old[3] & 1u)};
}
__device__ __forceinline__ unsigned changed4(u32x4 a, u32x4 old) { return ((a[0] ^ old[0]) & (a[1] ^ old[1])) & ((a[2] ^ old[2]) & (a[3] ^ old[3])); }

__device__ __forceinline__ f32x4 clr0(u32x4 u) { return __builtin_bit_cast(f32x4, u & ~1u); }
__device__ __forceinline__ float max4(f32x4 x) { return fmaxf(fmaxf(x[0], x[1]), fmaxf(x[2], x[3])); }

constexpr int NTHREADS = (NW + 1) * 64;
constexpr int NX = 16;
constexpr unsigned KV_BYTES = (unsigned)B * HKV * SK * D * 2, Q_BYTES = (unsigned)B * HQ * SQ * D * 2;

__global__ __launch_bounds__(NTHREADS) void attn_fwd(const void* __restrict__ q, const void* __restrict__ k, const void* __restrict__ v,
                                                     void* __restrict__ out, char* __restrict__ ws
) {
  extern __shared__ __attribute__((aligned(16))) char smem[];
  const bool lng = !(blockIdx.x & 1);
  const int tid = threadIdx.x, lane = tid & 63;
  const int w = __builtin_amdgcn_readfirstlane(tid >> 6);
  const int x8 = blockIdx.x & 7;
  const bool isP = x8 < 4;
  const int j4 = isP ? x8 : (x8 ^ 5);
  const int slot = (blockIdx.x >> 3) * 4 + j4;
  const int bh = j4 * 32 + (blockIdx.x >> 3);
  const int i = lane & 15, g = lane >> 4;
  const unsigned lds0 = (unsigned)(size_t)(LAS char*)smem;

  if (w == NW) {
    asm volatile("" ::"s"(q), "s"(k), "s"(v), "s"(out));
    char* const pair = ws + (size_t)bh * WS_PAIR;
    const auto prs = __builtin_amdgcn_make_buffer_rsrc((void*)pair, 0, WS_PAYLOAD, 0x00020000);
    LAS unsigned* const cw = (LAS unsigned*)((LAS char*)smem + LDS_CW);
    unsigned* const ctl = (unsigned*)(pair + WS_CTL);
    const int me = isP ? 1 : 0;
    if (lane < 12) cw[lane] = lane == 2 ? ROLE_UNKNOWN : 0u;
    unsigned cv;
    {
      const unsigned vo = lane * 16, vo4 = 4096 + lane * 16, vo8 = 8192 + lane * 16;
      asm volatile(
          "global_load_dword %0, %1, off sc1\n\t"
          "s_mov_b32 m0, %2\n\t"
          "s_nop 0\n\t"
          "buffer_load_dwordx4 %6, %5, 0 offen sc1 nt lds\n\t"
          "buffer_load_dwordx4 %6, %5, 0 offen offset:1024 sc1 nt lds\n\t"
          "buffer_load_dwordx4 %6, %5, 0 offen offset:2048 sc1 nt lds\n\t"
          "buffer_load_dwordx4 %6, %5, 0 offen offset:3072 sc1 nt lds\n\t"
          "s_mov_b32 m0, %3\n\t"
          "s_nop 0\n\t"
          "buffer_load_dwordx4 %7, %5, 0 offen sc1 nt lds\n\t"
          "buffer_load_dwordx4 %7, %5, 0 offen offset:1024 sc1 nt lds\n\t"
          "buffer_load_dwordx4 %7, %5, 0 offen offset:2048 sc1 nt lds\n\t"
          "buffer_load_dwordx4 %7, %5, 0 offen offset:3072 sc1 nt lds\n\t"
          "s_mov_b32 m0, %4\n\t"
          "s_nop 0\n\t"
          "buffer_load_dwordx4 %8, %5, 0 offen sc1 nt lds\n\t"
          "s_waitcnt lgkmcnt(0)\n\t"
          "s_barrier\n\t"
          "s_waitcnt vmcnt(0)"
          : "=&v"(cv)
          : "v"(ctl + min(lane, 4) * 32), "s"(lds0 + LDS_OLD), "s"(lds0 + LDS_OLD + 4096), "s"(lds0 + LDS_OLD + 8192), "s"(prs), "v"(vo), "v"(vo4), "v"(vo8)
          : "memory");
    }
    const unsigned hello_mine0 = __builtin_amdgcn_readlane(cv, CTL_HELLO + me), hello_peer0 = __builtin_amdgcn_readlane(cv, CTL_HELLO + 1 - me);
    const unsigned join_peer0 = __builtin_amdgcn_readlane(cv, CTL_JOIN + 1 - me), arr0 = __builtin_amdgcn_readlane(cv, CTL_ARR);
    unsigned hello = hello_mine0 + 1;
    if (hello == join_peer0) ++hello;
    if (lane == 0) st_agent(ctl + (CTL_HELLO + me) * 32, hello);
    bool dyn = false;
    const unsigned* const pollp = ctl + ((lane & 1) ? CTL_JOIN + 1 - me : CTL_HELLO + 1 - me) * 32;
    __builtin_amdgcn_s_sleep(POLL0);
    for (unsigned spin = 0; spin < SPIN_LIMIT; ++spin) {
      if (!dyn) {
        unsigned x = 0;
        if (lane < 2) x = ld_agent(pollp);
        const unsigned xh = __builtin_amdgcn_readlane(x, 0), xj = __builtin_amdgcn_readlane(x, 1);
        if (xh != hello_peer0) {
          dyn = true;
          if (lane == 0) st_agent(ctl + (CTL_JOIN + me) * 32, xh);
        } else if (xj == hello) {
          dyn = true;
        }
        if (!dyn) { __builtin_amdgcn_s_sleep(POLL1); continue; }
      }
      const u32x4 f = *(volatile LAS u32x4*)(cw + 8);
      if ((f[0] & f[1] & f[2] & f[3]) != 0) break;
      __builtin_amdgcn_s_sleep(4);
    }
    unsigned old = 0;
    if (lane == 0) old = __hip_atomic_fetch_add((GAS unsigned*)(ctl + CTL_ARR * 32), 1u, __ATOMIC_RELAXED, __HIP_MEMORY_SCOPE_AGENT);
    const unsigned role = __builtin_amdgcn_readfirstlane(old) == arr0 ? ROLE_PUBLISH : ROLE_COMBINE;
    if (lane == 0) *(volatile LAS unsigned*)(cw + 2) = role;
    wait_lgkm();
    return;
  }

  const int nr = lng ? 8 : 7;
  const unsigned bhoff = (unsigned)bh * (SK * D * 2);
  const unsigned ring0 = bhoff + (isP ? 0 : SK - nr * NW * TILE) * (D * 2);
  const unsigned ext0 = bhoff + ((isP ? nr * NW * TILE : SK - nr * NW * TILE - NW * NX) + w * NX) * (D * 2);
  const unsigned ring = lds0 + w * RING;

  unsigned kv[4], vv[2];
#pragma unroll
  for (int j = 0; j < 4; ++j) kv[j] = g * 256 + ((i ^ (4 * j + g)) * 16);
#pragma unroll
  for (int j = 0; j < 2; ++j) vv[j] = g * 256 + ((i ^ (8 * j + 2 * g)) * 16);
  const unsigned qv = w * 1024 + g * 256 + ((i ^ (4 * w + g)) * 16);
  const unsigned qso = (unsigned)bh * (16 * D * 2), qm0 = lds0 + LDS_Q + w * 1024, vm0 = ring + SLOT;

  asm volatile("" ::"v"(kv[0]), "v"(kv[1]), "v"(kv[2]), "v"(kv[3]), "v"(vv[0]), "v"(vv[1]), "v"(qv), "s"(qso), "s"(qm0), "s"(vm0), "s"(ring), "s"(ext0), "s"(ring0));
  asm volatile("" : "+s"(q), "+s"(k), "+s"(v));
  const auto krs = __builtin_amdgcn_make_buffer_rsrc((void*)k, 0, KV_BYTES, 0x00020000);
  const auto vrs = __builtin_amdgcn_make_buffer_rsrc((void*)v, 0, KV_BYTES, 0x00020000);
  const auto qrs = __builtin_amdgcn_make_buffer_rsrc((void*)q, 0, Q_BYTES, 0x00020000);

  auto issueK = [&](unsigned o) {
    dma4(ring, krs, o, kv[0], kv[1], kv[2], kv[3]);
    dma4(ring + 4096, krs, o + 4096, kv[0], kv[1], kv[2], kv[3]);
  };
  auto issueV = [&](unsigned o) {
    dma4(ring + SLOT, vrs, o, vv[0], vv[1], vv[0], vv[1]);
    dma4(ring + SLOT + 4096, vrs, o + 4096, vv[0], vv[1], vv[0], vv[1]);
  };
  auto tile_off = [&](int r) -> unsigned { return ring0 + (unsigned)(NW * r + ((w + r) & (NW - 1))) * (TILE * 256); };

  dma1(qm0, qrs, qso, qv);
  dma4(ring, krs, ext0, kv[0], kv[1], kv[2], kv[3]);
  dma4(vm0, vrs, ext0, vv[0], vv[1], vv[0], vv[1]);
  asm volatile("" : "+s"(ws), "+s"(out));
  const unsigned tau = lng ? (T_LAST_8 - T_FIRST) * 256 / 16 : (T_LAST_7 - T_FIRST) * 256 / 14;
  const unsigned tbase = (unsigned)__builtin_amdgcn_s_memrealtime() + T_FIRST - ((unsigned)(slot * T_SPREAD) >> 7);
  auto now = [&]() -> unsigned { return (unsigned)__builtin_amdgcn_s_memrealtime(); };
  auto pace = [&](int u, unsigned t) {
    const unsigned due = tbase + ((u * tau) >> 8);
#pragma unroll 1
    for (int z = 0; z < 4096 && (int)(t - due) < 0; ++z) {
      __builtin_amdgcn_s_sleep(1);
      t = (unsigned)__builtin_amdgcn_s_memrealtime();
    }
  };
  const auto prs = __builtin_amdgcn_make_buffer_rsrc((void*)(ws + (size_t)bh * WS_PAIR), 0, WS_PAYLOAD, 0x00020000);

  LAS char* const ringp = (LAS char*)smem + w * RING;
  unsigned ka[4];
#pragma unroll
  for (int s = 0; s < 4; ++s) ka[s] = i * 256 + (((4 * s) ^ g ^ i) * 16);
  const int q_ = i >> 2, p_ = i & 3;
  const unsigned va0 = (4 * g + q_) * 256 + (((8 * (g & 1) + 2 * q_) + (p_ >> 1)) * 16) + (p_ & 1) * 8;

  wait_vm<8>();
  __builtin_amdgcn_s_barrier();
  asm volatile("" ::: "memory");
  u32x4 Qf[4];
#pragma unroll
  for (int s = 0; s < 4; ++s) Qf[s] = *(LAS u32x4*)((LAS char*)smem + LDS_Q + ka[s]);

  f32x4 acc[8];
  float m, l;
  unsigned cur = tile_off(0);

  {
    u32x4 kf[4];
    wait_vm<4>();
#pragma unroll
    for (int s = 0; s < 4; ++s) kf[s] = *(LAS u32x4*)(ringp + ka[s]);
    wait_lgkm();
    __builtin_amdgcn_sched_barrier(0);
    issueK(cur);
    __builtin_amdgcn_sched_barrier(0);
    f32x4 s0 = {0.f, 0.f, 0.f, 0.f};
#pragma unroll
    for (int s = 0; s < 4; ++s) s0 = __builtin_amdgcn_mfma_f32_16x16x32_bf16(asbf(kf[s]), asbf(Qf[s]), s0, 0, 0, 0);
    m = xmax(max4(s0));
    const float mc = -m * CEXP;
    float p[4];
#pragma unroll
    for (int r = 0; r < 4; ++r) p[r] = __builtin_amdgcn_exp2f(fmaf(s0[r], CEXP, mc));
    l = (p[0] + p[1]) + (p[2] + p[3]);
    const u32x2 P = {cvtpk(p[0], p[1]), cvtpk(p[2], p[3])};
    __builtin_amdgcn_sched_barrier(0);
    wait_vm<8>();
    __builtin_amdgcn_sched_barrier(0);
    s16x4 vf[8];
#pragma unroll
    for (int cb = 0; cb < 8; ++cb) vf[cb] = __builtin_amdgcn_ds_read_tr16_b64_v4i16((LAS s16x4*)(ringp + SLOT + (va0 ^ (cb << 5))));
    wait_lgkm();
    __builtin_amdgcn_sched_barrier(0);
#pragma unroll
    for (int cb = 0; cb < 8; ++cb)
      acc[cb] = __builtin_amdgcn_mfma_f32_16x16x16bf16_1k(vf[cb], __builtin_bit_cast(s16x4, P), f32x4{0.f, 0.f, 0.f, 0.f}, 0, 0, 0);
  }

  auto scores = [&](f32x4& s0, f32x4& s1) {
    u32x4 kf[8];
#pragma unroll
    for (int s = 0; s < 4; ++s) {
      kf[2 * s] = *(LAS u32x4*)(ringp + ka[s]);
      kf[2 * s + 1] = *(LAS u32x4*)(ringp + 4096 + ka[s]);
    }
    wait_lgkm();
    __builtin_amdgcn_sched_barrier(0);
    s0 = f32x4{0.f, 0.f, 0.f, 0.f};
    s1 = s0;
#pragma unroll
    for (int s = 0; s < 4; ++s) {
      s0 = __builtin_amdgcn_mfma_f32_16x16x32_bf16(asbf(kf[2 * s]), asbf(Qf[s]), s0, 0, 0, 0);
      s1 = __builtin_amdgcn_mfma_f32_16x16x32_bf16(asbf(kf[2 * s + 1]), asbf(Qf[s]), s1, 0, 0, 0);
    }
  };
  auto probs = [&](f32x4 s0, f32x4 s1) {
    const float mc = -m * CEXP;
    float p[8];
#pragma unroll
    for (int r = 0; r < 4; ++r) {
      p[r] = __builtin_amdgcn_exp2f(fmaf(s0[r], CEXP, mc));
      p[4 + r] = __builtin_amdgcn_exp2f(fmaf(s1[r], CEXP, mc));
    }
    l += ((p[0] + p[1]) + (p[2] + p[3])) + ((p[4] + p[5]) + (p[6] + p[7]));
    return u32x4{cvtpk(p[0], p[1]), cvtpk(p[2], p[3]), cvtpk(p[4], p[5]), cvtpk(p[6], p[7])};
  };
  auto rescale = [&](float mn) {
    const float alpha = __builtin_amdgcn_exp2f((m - mn) * CEXP);
    l *= alpha;
#pragma unroll
    for (int e = 0; e < 8; ++e) acc[e] *= alpha;
    m = mn;
  };
  auto values = [&](u32x4 P, bool drain) {
    s16x4 vf[16];
#pragma unroll
    for (int cb = 0; cb < 8; ++cb) {
      const unsigned a = va0 ^ (cb << 5);
      vf[2 * cb] = __builtin_amdgcn_ds_read_tr16_b64_v4i16((LAS s16x4*)(ringp + SLOT + a));
      vf[2 * cb + 1] = __builtin_amdgcn_ds_read_tr16_b64_v4i16((LAS s16x4*)(ringp + SLOT + 4096 + a));
    }
    if (drain) wait_lgkm();
    __builtin_amdgcn_sched_barrier(0);
#pragma unroll
    for (int cb = 0; cb < 8; ++cb) {
      const s16x8 f = __builtin_shufflevector(vf[2 * cb], vf[2 * cb + 1], 0, 1, 2, 3, 4, 5, 6, 7);
      acc[cb] = __builtin_amdgcn_mfma_f32_16x16x32_bf16(__builtin_bit_cast(bf16x8, f), asbf(P), acc[cb], 0, 0, 0);
    }
  };

#pragma unroll 1
  for (int r = 1; r < nr; ++r) {
    if (nr - r == CKPT && lane == 0) *(volatile LAS unsigned*)((LAS char*)smem + LDS_CW + 32 + w * 4) = 1u;
    { const unsigned t = now(); wait_vm<AHEAD>(); pace(2 * r, t); }
    issueV(cur);
    wait_vm<8>();
    __builtin_amdgcn_sched_barrier(0);
    f32x4 s0, s1;
    scores(s0, s1);
    const float mn = fmaxf(m, xmax(fmaxf(max4(s0), max4(s1))));
    if (__builtin_amdgcn_ballot_w64(mn > m)) rescale(mn);
    const u32x4 P = probs(s0, s1);
    cur = tile_off(r);
    __builtin_amdgcn_sched_barrier(0);
    { const unsigned t = now(); wait_vm<AHEAD>(); pace(2 * r + 1, t); }
    issueK(cur);
    wait_vm<8>();
    __builtin_amdgcn_sched_barrier(0);
    values(P, true);
  }

  const int qq = tid >> 4, jj = tid & 15;
  LAS char* const sm = (LAS char*)smem;
  u32x4 ra0 = {0u, 0u, 0u, 0u}, ra1 = ra0, rst = ra0;
  f32x4 ta0 = {0.f, 0.f, 0.f, 0.f}, ta1 = ta0;
  float fb = 0.f, tl = 0.f;
  bool have = false;
  auto readRole = [&]() { return __builtin_amdgcn_readfirstlane(*(volatile LAS unsigned*)(sm + LDS_CW + 8)); };
  auto fetchLds = [&](int area) {
    const unsigned o0 = tid * 16, o1 = 4096 + tid * 16, o2 = WS_STATS + qq * 16, mb = lds0 + area + w * 3072;
    asm volatile(
        "s_mov_b32 m0, %0\n\t"
        "s_nop 0\n\t"
        "buffer_load_dwordx4 %4, %3, 0 offen sc1 lds\n\t"
        "s_mov_b32 m0, %1\n\t"
        "s_nop 0\n\t"
        "buffer_load_dwordx4 %5, %3, 0 offen sc1 lds\n\t"
        "s_mov_b32 m0, %2\n\t"
        "s_nop 0\n\t"
        "buffer_load_dwordx4 %6, %3, 0 offen sc1 lds"
        :
        : "s"(mb), "s"(mb + 1024), "s"(mb + 2048), "s"(prs), "v"(o0), "v"(o1), "v"(o2)
        : "memory");
  };

  unsigned role = readRole();
  { const unsigned t = now(); wait_vm<AHEAD>(); pace(2 * nr, t); }
  issueV(cur);
  wait_vm<8>();
  __builtin_amdgcn_sched_barrier(0);
  f32x4 s0, s1;
  scores(s0, s1);
  const float mn = fmaxf(m, xmax(fmaxf(max4(s0), max4(s1))));
  if (g == 0) *(LAS float*)(sm + LDS_SM + i * 16 + w * 4) = mn;
  wait_lgkm();
  __builtin_amdgcn_s_barrier();
  asm volatile("" ::: "memory");
  if (role == ROLE_UNKNOWN) role = readRole();
  const bool late = role == ROLE_COMBINE;
  if (late) fetchLds(LDS_E2);
  const f32x4 mw = *(LAS f32x4*)(sm + LDS_SM + i * 16), mq = *(LAS f32x4*)(sm + LDS_SM + qq * 16);
  const u32x4 old0 = *(LAS u32x4*)(sm + LDS_OLD + tid * 16), old1 = *(LAS u32x4*)(sm + LDS_OLD + 4096 + tid * 16);
  const u32x4 olds = *(LAS u32x4*)(sm + LDS_OLD + WS_STATS + qq * 16);
  rescale(max4(mw));
#pragma unroll
  for (int e = 0; e < 8; ++e) asm volatile("" : "+v"(acc[e]));
  const u32x4 P = probs(s0, s1);
  l = xsum(l);
  if (g == 0) *(LAS float*)(sm + LDS_SL + i * 16 + w * 4) = l;
  const float M = max4(mq);

  auto arrived = [&]() {
    const unsigned ch = changed4(ra0, old0) & changed4(ra1, old1) & changed4(rst, olds);
    return __builtin_amdgcn_ballot_w64((ch & 1u) == 0) == 0;
  };
  auto prepare = [&]() {
#pragma clang fp contract(off)
    const float MA = __uint_as_float((rst[0] & 0xffff0000u) | (rst[1] >> 16));
    const float Mt = fmaxf(M, MA);
    fb = __builtin_amdgcn_exp2f((M - Mt) * CEXP);
    const float fa = __builtin_amdgcn_exp2f((MA - Mt) * CEXP);
    tl = __uint_as_float(rst[2] & ~1u) * fa;
    ta0 = clr0(ra0) * fa;
    ta1 = clr0(ra1) * fa;
  };
  auto takeFetched = [&](int area) {
    LAS char* e = sm + area + w * 3072 + lane * 16;
    ra0 = *(LAS u32x4*)e;
    ra1 = *(LAS u32x4*)(e + 1024);
    rst = *(LAS u32x4*)(e + 2048);
    have = arrived();
    if (have) prepare();
  };
  GAS char* dst = (GAS char*)out + (size_t)(bh * 16 + qq) * (D * 2) + jj * 16;
  const unsigned mb = __float_as_uint(M);
  u32x4 stw = flip4(u32x4{mb & 0xffff0000u, mb << 16, 0u, 0u}, olds);
  unsigned wr = ring + i * OROW + (g & 1) * 256 + (g >> 1) * 16, rd = lds0 + qq * OROW + jj * 16, rl = lds0 + LDS_SL + qq * 16;
  unsigned so0 = tid * 16, so1 = 4096 + tid * 16, so2 = WS_STATS + qq * 16;
  asm volatile("" : "+v"(stw), "+v"(dst), "+v"(wr), "+v"(rd), "+v"(rl), "+v"(so0), "+v"(so1), "+v"(so2));
  __builtin_amdgcn_sched_barrier(0);
  if (late) wait_vm<3>(); else wait_vm<0>();
  __builtin_amdgcn_sched_barrier(0);
  values(P, false);
  {
    LAS char* d = (LAS char*)(size_t)wr;
#pragma unroll
    for (int cb = 0; cb < 8; ++cb) *(LAS f32x4*)(d + cb * 32) = acc[cb];
  }
  if (late) { wait_vm<0>(); takeFetched(LDS_E2); }
  const bool again = late && !have;
  if (again) fetchLds(LDS_E);
  wait_lgkm();
  __builtin_amdgcn_s_barrier();
  asm volatile("" ::: "memory");

  const f32x4 lw = *(LAS f32x4*)(size_t)rl;
  f32x4 pa[NW], pb[NW];
#pragma unroll
  for (int ww = 0; ww < NW; ++ww) {
    LAS char* src = (LAS char*)(size_t)rd + ww * RING;
    pa[ww] = *(LAS f32x4*)src;
    pb[ww] = *(LAS f32x4*)(src + 256);
  }
  const float L = (lw[0] + lw[1]) + (lw[2] + lw[3]);
  const f32x4 o0 = (pa[0] + pa[1]) + (pa[2] + pa[3]), o1 = (pb[0] + pb[1]) + (pb[2] + pb[3]);

  if (role != ROLE_COMBINE) {
    __builtin_amdgcn_raw_buffer_store_b128(flip4(asu(o0), old0), prs, so0, 0, 16);
    __builtin_amdgcn_raw_buffer_store_b128(flip4(asu(o1), old1), prs, so1, 0, 16);
    if (jj == 0) {
      stw[2] |= __float_as_uint(L) & ~1u;
      __builtin_amdgcn_raw_buffer_store_b128(stw, prs, so2, 0, 16);
    }
  } else {
    wait_vm<0>();
    if (again) takeFetched(LDS_E);
    if (!have) {
      for (unsigned spin = 0; spin < SPIN_LIMIT; ++spin) {
        ra0 = __builtin_amdgcn_raw_buffer_load_b128(prs, so0, 0, 16);
        ra1 = __builtin_amdgcn_raw_buffer_load_b128(prs, so1, 0, 16);
        rst = __builtin_amdgcn_raw_buffer_load_b128(prs, so2, 0, 16);
        if (arrived()) break;
        __builtin_amdgcn_s_sleep(2);
      }
      prepare();
    }
    {
#pragma clang fp contract(off)
      const float inv = __builtin_amdgcn_rcpf(__uint_as_float(__float_as_uint(L) & ~1u) * fb + tl);
      const f32x4 r0 = (clr0(asu(o0)) * fb + ta0) * inv, r1 = (clr0(asu(o1)) * fb + ta1) * inv;
      const u32x4 o = {cvtpk(r0[0], r0[1]), cvtpk(r0[2], r0[3]), cvtpk(r1[0], r1[1]), cvtpk(r1[2], r1[3])};
      *(GAS u32x4*)dst = o;
    }
  }
}

extern "C" void kernel_launch(void* const* d_in, const int* in_sizes, int n_in, void* d_out, int out_size, void* d_ws,
                              size_t ws_size, hipStream_t stream) {
  (void)hipFuncSetAttribute((const void*)attn_fwd, hipFuncAttributeMaxDynamicSharedMemorySize, LDS_BYTES);
  attn_fwd<<<NBH * 2, NTHREADS, LDS_BYTES, stream>>>(d_in[0], d_in[1], d_in[2], d_out, (char*)d_ws);
}
```

```cpp
#include <hip/hip_runtime.h>
#include <hip/hip_bf16.h>
#include <stdint.h>

typedef __attribute__((ext_vector_type(4))) unsigned u32x4;
typedef __attribute__((ext_vector_type(2))) unsigned u32x2;
typedef __attribute__((ext_vector_type(4))) float f32x4;
typedef __attribute__((ext_vector_type(4))) short s16x4;
typedef __attribute__((ext_vector_type(8))) short s16x8;
typedef __attribute__((ext_vector_type(8))) __bf16 bf16x8;
#define LAS __attribute__((address_space(3)))
#define GAS __attribute__((address_space(1)))

constexpr int B = 16, HQ = 32, HKV = 8, SQ = 4, SK = 2048, D = 128;
constexpr int NBH = B * HKV;
constexpr int NW = 8;
constexpr int TILE = 32;
constexpr int NR = SK / (NW * TILE);
constexpr float CEXP = 0.08838834764831845f * 1.4426950408889634f;
constexpr int KSLOT = 8192, VSLOT = 4096, RING = KSLOT + VSLOT;
constexpr int LDS_Q = NW * RING;
constexpr int LDS_SM = LDS_Q + 4096;
constexpr int LDS_SL = LDS_SM + 512;
constexpr int LDS_BYTES = LDS_SL + 512;
constexpr int OROW = 272;
constexpr int AHEAD_K = 4;
constexpr int AHEAD_V = 2;
constexpr unsigned T_LAST = 1700, T_SPREAD = 100;
constexpr unsigned TAU = T_LAST * 256 / (2 * NR - 1);
static_assert((HQ / HKV) * SQ == 16, "16 packed query rows per kv head");
static_assert(16 * OROW <= RING, "LDS layout");

__device__ __forceinline__ unsigned cvtpk(float lo, float hi) {
  unsigned r;
  asm("v_cvt_pk_bf16_f32 %0, %1, %2" : "=v"(r) : "v"(lo), "v"(hi));
  return r;
}
__device__ __forceinline__ float xmax(float x) {
  auto s = __builtin_amdgcn_permlane16_swap(__float_as_uint(x), __float_as_uint(x), false, false);
  x = fmaxf(__uint_as_float(s[0]), __uint_as_float(s[1]));
  auto t = __builtin_amdgcn_permlane32_swap(__float_as_uint(x), __float_as_uint(x), false, false);
  return fmaxf(__uint_as_float(t[0]), __uint_as_float(t[1]));
}
__device__ __forceinline__ float xsum(float x) {
  auto s = __builtin_amdgcn_permlane16_swap(__float_as_uint(x), __float_as_uint(x), false, false);
  x = __uint_as_float(s[0]) + __uint_as_float(s[1]);
  auto t = __builtin_amdgcn_permlane32_swap(__float_as_uint(x), __float_as_uint(x), false, false);
  return __uint_as_float(t[0]) + __uint_as_float(t[1]);
}
__device__ __forceinline__ bf16x8 asbf(u32x4 v) { return __builtin_bit_cast(bf16x8, v); }

__device__ __forceinline__ void dma4(unsigned m0v, __amdgpu_buffer_rsrc_t srd, unsigned soff, unsigned v0, unsigned v1,
                                     unsigned v2, unsigned v3) {
  asm volatile(
      "s_mov_b32 m0, %0\n\t"
      "s_nop 0\n\t"
      "buffer_load_dwordx4 %3, %1, %2 offen nt lds\n\t"
      "buffer_load_dwordx4 %4, %1, %2 offen offset:1024 nt lds\n\t"
      "buffer_load_dwordx4 %5, %1, %2 offen offset:2048 nt lds\n\t"
      "buffer_load_dwordx4 %6, %1, %2 offen offset:3072 nt lds"
      :
      : "s"(m0v), "s"(srd), "s"(soff), "v"(v0), "v"(v1), "v"(v2), "v"(v3)
      : "memory");
}
__device__ __forceinline__ void dma1(unsigned m0v, __amdgpu_buffer_rsrc_t srd, unsigned soff, unsigned v0) {
  asm volatile(
      "s_mov_b32 m0, %0\n\t"
      "s_nop 0\n\t"
      "buffer_load_dwordx4 %3, %1, %2 offen nt lds"
      :
      : "s"(m0v), "s"(srd), "s"(soff), "v"(v0)
      : "memory");
}
template <int N>
__device__ __forceinline__ void wait_vm() { asm volatile("s_waitcnt vmcnt(%0)" ::"i"(N) : "memory"); }
__device__ __forceinline__ void wait_lgkm() { asm volatile("s_waitcnt lgkmcnt(0)" ::: "memory"); }
__device__ __forceinline__ float max4(f32x4 x) { return fmaxf(fmaxf(x[0], x[1]), fmaxf(x[2], x[3])); }
__device__ __forceinline__ float sum4(f32x4 x) { return (x[0] + x[1]) + (x[2] + x[3]); }

constexpr int NTHREADS = NW * 64;
constexpr unsigned KV_BYTES = (unsigned)B * HKV * SK * D * 2, Q_BYTES = (unsigned)B * HQ * SQ * D * 2;

__global__ __launch_bounds__(NTHREADS) void attn_fwd(const void* __restrict__ q, const void* __restrict__ k, const void* __restrict__ v,
                                                     void* __restrict__ out) {
  extern __shared__ __attribute__((aligned(16))) char smem[];
  const int tid = threadIdx.x, lane = tid & 63;
  const int w = __builtin_amdgcn_readfirstlane(tid >> 6);
  const int x8 = blockIdx.x & 7, j8 = blockIdx.x >> 3;
  const int half = j8 & 1;
  const int bh = (j8 >> 2) * 16 + x8 * 2 + ((j8 >> 1) & 1);
  const int i = lane & 15, g = lane >> 4;
  const unsigned lds0 = (unsigned)(size_t)(LAS char*)smem;
  const unsigned bhoff = (unsigned)bh * (SK * D * 2);
  const unsigned ring = lds0 + w * RING;

  unsigned kv[4], vv[4];
#pragma unroll
  for (int j = 0; j < 4; ++j) kv[j] = g * 256 + ((i ^ (4 * j + g)) * 16);
  {
    const int rr = lane >> 3, c = lane & 7;
#pragma unroll
    for (int j = 0; j < 4; ++j) vv[j] = rr * 256 + half * 128 + ((c ^ (rr & 6)) * 16) + 1024 * j;
  }
  const unsigned qv = (w & 3) * 1024 + g * 256 + ((i ^ (4 * (w & 3) + g)) * 16);
  const unsigned qso = (unsigned)bh * (16 * D * 2), qm0 = lds0 + LDS_Q + (w & 3) * 1024, vm0 = ring + KSLOT;
  auto tile_off = [&](int r) -> unsigned { return bhoff + (unsigned)(NW * r + ((w + r) & (NW - 1))) * (TILE * 256); };
  unsigned cur = tile_off(0);

  asm volatile("" ::"v"(kv[0]), "v"(kv[1]), "v"(kv[2]), "v"(kv[3]), "v"(vv[0]), "v"(vv[1]), "v"(vv[2]), "v"(vv[3]), "v"(qv), "s"(qso), "s"(qm0), "s"(vm0), "s"(ring), "s"(cur));
  asm volatile("" : "+s"(q), "+s"(k), "+s"(v));
  const auto krs = __builtin_amdgcn_make_buffer_rsrc((void*)k, 0, KV_BYTES, 0x00020000);
  const auto vrs = __builtin_amdgcn_make_buffer_rsrc((void*)v, 0, KV_BYTES, 0x00020000);
  const auto qrs = __builtin_amdgcn_make_buffer_rsrc((void*)q, 0, Q_BYTES, 0x00020000);

  auto issueK = [&](unsigned o) {
    dma4(ring, krs, o, kv[0], kv[1], kv[2], kv[3]);
    dma4(ring + 4096, krs, o + 4096, kv[0], kv[1], kv[2], kv[3]);
  };
  auto issueV = [&](unsigned o) { dma4(vm0, vrs, o, vv[0], vv[1], vv[2], vv[3]); };

  if (w < 4) dma1(qm0, qrs, qso, qv);
  issueK(cur);
  issueV(cur);
  asm volatile("" : "+s"(out));
  const unsigned tbase = (unsigned)__builtin_amdgcn_s_memrealtime() - ((unsigned)(bh * T_SPREAD) >> 7);
  auto now = [&]() -> unsigned { return (unsigned)__builtin_amdgcn_s_memrealtime(); };
  auto pace = [&](int u, unsigned t) {
    const unsigned due = tbase + ((u * TAU) >> 8);
#pragma unroll 1
    for (int z = 0; z < 4096 && (int)(t - due) < 0; ++z) {
      __builtin_amdgcn_s_sleep(1);
      t = (unsigned)__builtin_amdgcn_s_memrealtime();
    }
  };

  LAS char* const ringp = (LAS char*)smem + w * RING;
  unsigned ka[4];
#pragma unroll
  for (int s = 0; s < 4; ++s) ka[s] = i * 256 + (((4 * s) ^ g ^ i) * 16);
  const int q_ = i >> 2, p_ = i & 3;
  const unsigned va0 = (4 * g + q_) * 128 + ((4 * (g & 1) + 2 * (q_ >> 1) + (p_ >> 1)) * 16) + (p_ & 1) * 8;

  wait_vm<12>();
  __builtin_amdgcn_s_barrier();
  asm volatile("" ::: "memory");
  u32x4 Qf[4];
#pragma unroll
  for (int s = 0; s < 4; ++s) Qf[s] = *(LAS u32x4*)((LAS char*)smem + LDS_Q + ka[s]);

  f32x4 acc[4];
#pragma unroll
  for (int e = 0; e < 4; ++e) acc[e] = f32x4{0.f, 0.f, 0.f, 0.f};
  float m = -__builtin_inff(), l = 0.f;

  auto scores = [&](f32x4& s0, f32x4& s1) {
    u32x4 kf[8];
#pragma unroll
    for (int s = 0; s < 4; ++s) {
      kf[2 * s] = *(LAS u32x4*)(ringp + ka[s]);
      kf[2 * s + 1] = *(LAS u32x4*)(ringp + 4096 + ka[s]);
    }
    wait_lgkm();
    __builtin_amdgcn_sched_barrier(0);
    s0 = f32x4{0.f, 0.f, 0.f, 0.f};
    s1 = s0;
#pragma unroll
    for (int s = 0; s < 4; ++s) {
      s0 = __builtin_amdgcn_mfma_f32_16x16x32_bf16(asbf(kf[2 * s]), asbf(Qf[s]), s0, 0, 0, 0);
      s1 = __builtin_amdgcn_mfma_f32_16x16x32_bf16(asbf(kf[2 * s + 1]), asbf(Qf[s]), s1, 0, 0, 0);
    }
  };
  auto probs = [&](f32x4 s0, f32x4 s1) {
    const float mc = -m * CEXP;
    float p[8];
#pragma unroll
    for (int r = 0; r < 4; ++r) {
      p[r] = __builtin_amdgcn_exp2f(fmaf(s0[r], CEXP, mc));
      p[4 + r] = __builtin_amdgcn_exp2f(fmaf(s1[r], CEXP, mc));
    }
    l += ((p[0] + p[1]) + (p[2] + p[3])) + ((p[4] + p[5]) + (p[6] + p[7]));
    return u32x4{cvtpk(p[0], p[1]), cvtpk(p[2], p[3]), cvtpk(p[4], p[5]), cvtpk(p[6], p[7])};
  };
  auto rescale = [&](float mn) {
    const float alpha = __builtin_amdgcn_exp2f((m - mn) * CEXP);
    l *= alpha;
#pragma unroll
    for (int e = 0; e < 4; ++e) acc[e] *= alpha;
    m = mn;
  };
  auto values = [&](u32x4 P) {
    s16x4 vf[8];
#pragma unroll
    for (int cb = 0; cb < 4; ++cb) {
      const unsigned a = va0 ^ (cb << 5);
      vf[2 * cb] = __builtin_amdgcn_ds_read_tr16_b64_v4i16((LAS s16x4*)(ringp + KSLOT + a));
      vf[2 * cb + 1] = __builtin_amdgcn_ds_read_tr16_b64_v4i16((LAS s16x4*)(ringp + KSLOT + 2048 + a));
    }
    wait_lgkm();
    __builtin_amdgcn_sched_barrier(0);
#pragma unroll
    for (int cb = 0; cb < 4; ++cb) {
      const s16x8 f = __builtin_shufflevector(vf[2 * cb], vf[2 * cb + 1], 0, 1, 2, 3, 4, 5, 6, 7);
      acc[cb] = __builtin_amdgcn_mfma_f32_16x16x32_bf16(__builtin_bit_cast(bf16x8, f), asbf(P), acc[cb], 0, 0, 0);
    }
  };

#pragma unroll 1
  for (int r = 1; r < NR; ++r) {
    wait_vm<4>();
    __builtin_amdgcn_sched_barrier(0);
    f32x4 s0, s1;
    scores(s0, s1);
    const float mn = fmaxf(m, xmax(fmaxf(max4(s0), max4(s1))));
    if (__builtin_amdgcn_ballot_w64(mn > m)) rescale(mn);
    const u32x4 P = probs(s0, s1);
    cur = tile_off(r);
    __builtin_amdgcn_sched_barrier(0);
    { const unsigned t = now(); wait_vm<AHEAD_V>(); pace(2 * r, t); }
    issueK(cur);
    wait_vm<8>();
    __builtin_amdgcn_sched_barrier(0);
    values(P);
    { const unsigned t = now(); wait_vm<AHEAD_K>(); pace(2 * r + 1, t); }
    issueV(cur);
  }

  LAS char* const sm = (LAS char*)smem;
  wait_vm<4>();
  __builtin_amdgcn_sched_barrier(0);
  f32x4 s0, s1;
  scores(s0, s1);
  const float mn = fmaxf(m, xmax(fmaxf(max4(s0), max4(s1))));
  if (g == 0) *(LAS float*)(sm + LDS_SM + i * 32 + w * 4) = mn;
  wait_lgkm();
  __builtin_amdgcn_s_barrier();
  asm volatile("" ::: "memory");
  const f32x4 mw0 = *(LAS f32x4*)(sm + LDS_SM + i * 32), mw1 = *(LAS f32x4*)(sm + LDS_SM + i * 32 + 16);
  rescale(fmaxf(max4(mw0), max4(mw1)));
#pragma unroll
  for (int e = 0; e < 4; ++e) asm volatile("" : "+v"(acc[e]));
  const u32x4 P = probs(s0, s1);
  l = xsum(l);
  if (g == 0) *(LAS float*)(sm + LDS_SL + i * 32 + w * 4) = l;
  const int qq = (tid >> 4) & 15, jj = tid & 15;
  GAS char* dst = (GAS char*)out + (size_t)(bh * 16 + qq) * (D * 2) + half * 128 + jj * 8;
  unsigned wr = ring + i * OROW + g * 16, rd = lds0 + qq * OROW + jj * 16, rl = lds0 + LDS_SL + qq * 32;
  asm volatile("" : "+v"(dst), "+v"(wr), "+v"(rd), "+v"(rl));
  __builtin_amdgcn_sched_barrier(0);
  wait_vm<0>();
  __builtin_amdgcn_sched_barrier(0);
  values(P);
  {
    LAS char* d = (LAS char*)(size_t)wr;
#pragma unroll
    for (int cb = 0; cb < 4; ++cb) *(LAS f32x4*)(d + cb * 64) = acc[cb];
  }
  wait_lgkm();
  __builtin_amdgcn_s_barrier();
  asm volatile("" ::: "memory");

  if (w < 4) {
    const f32x4 lw0 = *(LAS f32x4*)(size_t)rl, lw1 = *(LAS f32x4*)(size_t)(rl + 16);
    f32x4 pa[NW];
#pragma unroll
    for (int ww = 0; ww < NW; ++ww) pa[ww] = *(LAS f32x4*)((LAS char*)(size_t)rd + ww * RING);
    const float inv = __builtin_amdgcn_rcpf(sum4(lw0) + sum4(lw1));
    const f32x4 o = (((pa[0] + pa[1]) + (pa[2] + pa[3])) + ((pa[4] + pa[5]) + (pa[6] + pa[7]))) * inv;
    *(GAS u32x2*)dst = u32x2{cvtpk(o[0], o[1]), cvtpk(o[2], o[3])};
  }
}

extern "C" void kernel_launch(void* const* d_in, const int* in_sizes, int n_in, void* d_out, int out_size, void* d_ws,
                              size_t ws_size, hipStream_t stream) {
  (void)hipFuncSetAttribute((const void*)attn_fwd, hipFuncAttributeMaxDynamicSharedMemorySize, LDS_BYTES);
  attn_fwd<<<NBH * 2, NTHREADS, LDS_BYTES, stream>>>(d_in[0], d_in[1], d_in[2], d_out);
}
```

```cpp
#include <hip/hip_runtime.h>
#include <hip/hip_bf16.h>
#include <stdint.h>
#include <stdio.h>

typedef __attribute__((ext_vector_type(4))) unsigned u32x4;
typedef __attribute__((ext_vector_type(2))) unsigned u32x2;
typedef __attribute__((ext_vector_type(4))) float f32x4;
typedef __attribute__((ext_vector_type(4))) short s16x4;
typedef __attribute__((ext_vector_type(8))) short s16x8;
typedef __attribute__((ext_vector_type(8))) __bf16 bf16x8;
#define LAS __attribute__((address_space(3)))
#define GAS __attribute__((address_space(1)))

constexpr int B = 16, HQ = 32, HKV = 8, SQ = 4, SK = 2048, D = 128;
constexpr int NBH = B * HKV;
constexpr int NW = 8;
constexpr int TILE = 32;
constexpr int NR = SK / (NW * TILE);
constexpr float CEXP = 0.08838834764831845f * 1.4426950408889634f;
constexpr int KSLOT = 8192, VSLOT = 4096, RING = KSLOT + VSLOT;
constexpr int LDS_Q = NW * RING;
constexpr int LDS_SM = LDS_Q + 4096;
constexpr int LDS_SL = LDS_SM + 512;
constexpr int LDS_BYTES = LDS_SL + 512;
constexpr int OROW = 272;
constexpr int AHEAD_K = 4;
constexpr int AHEAD_V = 2;
constexpr unsigned T_LAST = 1700;
constexpr unsigned T_LEAD = 219;
constexpr unsigned T_WAVE = 24;
constexpr int U_CLOSE = 9;
constexpr unsigned TAU = T_LAST * 256 / (2 * NR - 1);
static_assert((HQ / HKV) * SQ == 16, "16 packed query rows per kv head");
static_assert(16 * OROW <= RING, "LDS layout");

__device__ __forceinline__ unsigned cvtpk(float lo, float hi) {
  unsigned r;
  asm("v_cvt_pk_bf16_f32 %0, %1, %2" : "=v"(r) : "v"(lo), "v"(hi));
  return r;
}
__device__ __forceinline__ float xmax(float x) {
  auto s = __builtin_amdgcn_permlane16_swap(__float_as_uint(x), __float_as_uint(x), false, false);
  x = fmaxf(__uint_as_float(s[0]), __uint_as_float(s[1]));
  auto t = __builtin_amdgcn_permlane32_swap(__float_as_uint(x), __float_as_uint(x), false, false);
  return fmaxf(__uint_as_float(t[0]), __uint_as_float(t[1]));
}
__device__ __forceinline__ float xsum(float x) {
  auto s = __builtin_amdgcn_permlane16_swap(__float_as_uint(x), __float_as_uint(x), false, false);
  x = __uint_as_float(s[0]) + __uint_as_float(s[1]);
  auto t = __builtin_amdgcn_permlane32_swap(__float_as_uint(x), __float_as_uint(x), false, false);
  return __uint_as_float(t[0]) + __uint_as_float(t[1]);
}
__device__ __forceinline__ bf16x8 asbf(u32x4 v) { return __builtin_bit_cast(bf16x8, v); }

__device__ __forceinline__ void dma4(unsigned m0v, __amdgpu_buffer_rsrc_t srd, unsigned soff, unsigned v0, unsigned v1,
                                     unsigned v2, unsigned v3) {
  asm volatile(
      "s_mov_b32 m0, %0\n\t"
      "s_nop 0\n\t"
      "buffer_load_dwordx4 %3, %1, %2 offen nt lds\n\t"
      "buffer_load_dwordx4 %4, %1, %2 offen offset:1024 nt lds\n\t"
      "buffer_load_dwordx4 %5, %1, %2 offen offset:2048 nt lds\n\t"
      "buffer_load_dwordx4 %6, %1, %2 offen offset:3072 nt lds"
      :
      : "s"(m0v), "s"(srd), "s"(soff), "v"(v0), "v"(v1), "v"(v2), "v"(v3)
      : "memory");
}
__device__ __forceinline__ void dma1(unsigned m0v, __amdgpu_buffer_rsrc_t srd, unsigned soff, unsigned v0) {
  asm volatile(
      "s_mov_b32 m0, %0\n\t"
      "s_nop 0\n\t"
      "buffer_load_dwordx4 %3, %1, %2 offen nt lds"
      :
      : "s"(m0v), "s"(srd), "s"(soff), "v"(v0)
      : "memory");
}
template <int N>
__device__ __forceinline__ void wait_vm() { asm volatile("s_waitcnt vmcnt(%0)" ::"i"(N) : "memory"); }
__device__ __forceinline__ void wait_lgkm() { asm volatile("s_waitcnt lgkmcnt(0)" ::: "memory"); }
__device__ __forceinline__ float max4(f32x4 x) { return fmaxf(fmaxf(x[0], x[1]), fmaxf(x[2], x[3])); }
__device__ __forceinline__ float sum4(f32x4 x) { return (x[0] + x[1]) + (x[2] + x[3]); }

constexpr int NTHREADS = NW * 64;
constexpr unsigned KV_BYTES = (unsigned)B * HKV * SK * D * 2, Q_BYTES = (unsigned)B * HQ * SQ * D * 2;

__global__ __launch_bounds__(NTHREADS) void attn_fwd(const void* __restrict__ q, const void* __restrict__ k, const void* __restrict__ v,
                                                     void* __restrict__ out) {
  extern __shared__ __attribute__((aligned(16))) char smem[];
  const int tid = threadIdx.x, lane = tid & 63;
  const int w = __builtin_amdgcn_readfirstlane(tid >> 6);
  const int x8 = blockIdx.x & 7, j8 = blockIdx.x >> 3;
  const int half = j8 & 1;
  const int bh = (j8 >> 2) * 16 + x8 * 2 + ((j8 >> 1) & 1);
  const int i = lane & 15, g = lane >> 4;
  const unsigned lds0 = (unsigned)(size_t)(LAS char*)smem;
  const unsigned bhoff = (unsigned)bh * (SK * D * 2);
  const unsigned ring = lds0 + w * RING;

  unsigned kv[4], vv[4];
#pragma unroll
  for (int j = 0; j < 4; ++j) kv[j] = g * 256 + ((i ^ (4 * j + g)) * 16);
  {
    const int rr = lane >> 3, c = lane & 7;
#pragma unroll
    for (int j = 0; j < 4; ++j) vv[j] = rr * 256 + half * 128 + ((c ^ (rr & 6)) * 16) + 1024 * j;
  }
  const unsigned qv = (w & 3) * 1024 + g * 256 + ((i ^ (4 * (w & 3) + g)) * 16);
  const unsigned qso = (unsigned)bh * (16 * D * 2), qm0 = lds0 + LDS_Q + (w & 3) * 1024, vm0 = ring + KSLOT;
  auto tile_off = [&](int r) -> unsigned { return bhoff + (unsigned)(NW * r + ((w + r) & (NW - 1))) * (TILE * 256); };
  unsigned cur = tile_off(0);

  asm volatile("" ::"v"(kv[0]), "v"(kv[1]), "v"(kv[2]), "v"(kv[3]), "v"(vv[0]), "v"(vv[1]), "v"(vv[2]), "v"(vv[3]), "v"(qv), "s"(qso), "s"(qm0), "s"(vm0), "s"(ring), "s"(cur));
  asm volatile("" : "+s"(q), "+s"(k), "+s"(v));
  const auto krs = __builtin_amdgcn_make_buffer_rsrc((void*)k, 0, KV_BYTES, 0x00020000);
  const auto vrs = __builtin_amdgcn_make_buffer_rsrc((void*)v, 0, KV_BYTES, 0x00020000);
  const auto qrs = __builtin_amdgcn_make_buffer_rsrc((void*)q, 0, Q_BYTES, 0x00020000);

  auto issueK = [&](unsigned o) {
    dma4(ring, krs, o, kv[0], kv[1], kv[2], kv[3]);
    dma4(ring + 4096, krs, o + 4096, kv[0], kv[1], kv[2], kv[3]);
  };
  auto issueV = [&](unsigned o) { dma4(vm0, vrs, o, vv[0], vv[1], vv[2], vv[3]); };

  if (w < 4) dma1(qm0, qrs, qso, qv);
  issueK(cur);
  issueV(cur);
  asm volatile("" : "+s"(out));
  unsigned tbase;
  auto now = [&]() -> unsigned { return (unsigned)__builtin_amdgcn_s_memrealtime(); };
  auto pace = [&](int u, unsigned t) {
    const unsigned due = tbase + ((u * TAU) >> 8) - (2 * w - (NW - 1)) * (u > U_CLOSE ? u - U_CLOSE : 0);
#pragma unroll 1
    for (int z = 0; z < 4096 && (int)(t - due) < 0; ++z) {
      __builtin_amdgcn_s_sleep(1);
      t = (unsigned)__builtin_amdgcn_s_memrealtime();
    }
  };

  LAS char* const ringp = (LAS char*)smem + w * RING;
  unsigned ka[4];
#pragma unroll
  for (int s = 0; s < 4; ++s) ka[s] = i * 256 + (((4 * s) ^ g ^ i) * 16);
  const int q_ = i >> 2, p_ = i & 3;
  const unsigned va0 = (4 * g + q_) * 128 + ((4 * (g & 1) + 2 * (q_ >> 1) + (p_ >> 1)) * 16) + (p_ & 1) * 8;

  wait_vm<12>();
  __builtin_amdgcn_s_barrier();
  tbase = now() - T_LEAD + w * T_WAVE;
  asm volatile("" ::: "memory");
  u32x4 Qf[4];
#pragma unroll
  for (int s = 0; s < 4; ++s) Qf[s] = *(LAS u32x4*)((LAS char*)smem + LDS_Q + ka[s]);

  f32x4 acc[4];
#pragma unroll
  for (int e = 0; e < 4; ++e) acc[e] = f32x4{0.f, 0.f, 0.f, 0.f};
  float m = -__builtin_inff(), l = 0.f;

  auto scores = [&](f32x4& s0, f32x4& s1) {
    u32x4 kf[8];
#pragma unroll
    for (int s = 0; s < 4; ++s) {
      kf[2 * s] = *(LAS u32x4*)(ringp + ka[s]);
      kf[2 * s + 1] = *(LAS u32x4*)(ringp + 4096 + ka[s]);
    }
    wait_lgkm();
    __builtin_amdgcn_sched_barrier(0);
    s0 = f32x4{0.f, 0.f, 0.f, 0.f};
    s1 = s0;
#pragma unroll
    for (int s = 0; s < 4; ++s) {
      s0 = __builtin_amdgcn_mfma_f32_16x16x32_bf16(asbf(kf[2 * s]), asbf(Qf[s]), s0, 0, 0, 0);
      s1 = __builtin_amdgcn_mfma_f32_16x16x32_bf16(asbf(kf[2 * s + 1]), asbf(Qf[s]), s1, 0, 0, 0);
    }
  };
  auto probs = [&](f32x4 s0, f32x4 s1) {
    const float mc = -m * CEXP;
    float p[8];
#pragma unroll
    for (int r = 0; r < 4; ++r) {
      p[r] = __builtin_amdgcn_exp2f(fmaf(s0[r], CEXP, mc));
      p[4 + r] = __builtin_amdgcn_exp2f(fmaf(s1[r], CEXP, mc));
    }
    l += ((p[0] + p[1]) + (p[2] + p[3])) + ((p[4] + p[5]) + (p[6] + p[7]));
    return u32x4{cvtpk(p[0], p[1]), cvtpk(p[2], p[3]), cvtpk(p[4], p[5]), cvtpk(p[6], p[7])};
  };
  auto rescale = [&](float mn) {
    const float alpha = __builtin_amdgcn_exp2f((m - mn) * CEXP);
    l *= alpha;
#pragma unroll
    for (int e = 0; e < 4; ++e) acc[e] *= alpha;
    m = mn;
  };
  auto values = [&](u32x4 P) {
    s16x4 vf[8];
#pragma unroll
    for (int cb = 0; cb < 4; ++cb) {
      const unsigned a = va0 ^ (cb << 5);
      vf[2 * cb] = __builtin_amdgcn_ds_read_tr16_b64_v4i16((LAS s16x4*)(ringp + KSLOT + a));
      vf[2 * cb + 1] = __builtin_amdgcn_ds_read_tr16_b64_v4i16((LAS s16x4*)(ringp + KSLOT + 2048 + a));
    }
    wait_lgkm();
    __builtin_amdgcn_sched_barrier(0);
#pragma unroll
    for (int cb = 0; cb < 4; ++cb) {
      const s16x8 f = __builtin_shufflevector(vf[2 * cb], vf[2 * cb + 1], 0, 1, 2, 3, 4, 5, 6, 7);
      acc[cb] = __builtin_amdgcn_mfma_f32_16x16x32_bf16(__builtin_bit_cast(bf16x8, f), asbf(P), acc[cb], 0, 0, 0);
    }
  };

#pragma unroll 1
  for (int r = 1; r < NR; ++r) {
    wait_vm<4>();
    __builtin_amdgcn_sched_barrier(0);
    f32x4 s0, s1;
    scores(s0, s1);
    const float mn = fmaxf(m, xmax(fmaxf(max4(s0), max4(s1))));
    if (__builtin_amdgcn_ballot_w64(mn > m)) rescale(mn);
    const u32x4 P = probs(s0, s1);
    cur = tile_off(r);
    __builtin_amdgcn_sched_barrier(0);
    { const unsigned t = now(); wait_vm<AHEAD_V>(); pace(2 * r, t); }
    issueK(cur);
    wait_vm<8>();
    __builtin_amdgcn_sched_barrier(0);
    values(P);
    { const unsigned t = now(); wait_vm<AHEAD_K>(); pace(2 * r + 1, t); }
    issueV(cur);
  }

  LAS char* const sm = (LAS char*)smem;
  wait_vm<4>();
  __builtin_amdgcn_sched_barrier(0);
  f32x4 s0, s1;
  scores(s0, s1);
  const float mn = fmaxf(m, xmax(fmaxf(max4(s0), max4(s1))));
  if (g == 0) *(LAS float*)(sm + LDS_SM + i * 32 + w * 4) = mn;
  wait_lgkm();
  __builtin_amdgcn_s_barrier();
  asm volatile("" ::: "memory");
  const f32x4 mw0 = *(LAS f32x4*)(sm + LDS_SM + i * 32), mw1 = *(LAS f32x4*)(sm + LDS_SM + i * 32 + 16);
  rescale(fmaxf(max4(mw0), max4(mw1)));
#pragma unroll
  for (int e = 0; e < 4; ++e) asm volatile("" : "+v"(acc[e]));
  const u32x4 P = probs(s0, s1);
  l = xsum(l);
  if (g == 0) *(LAS float*)(sm + LDS_SL + i * 32 + w * 4) = l;
  const int qq = (tid >> 4) & 15, jj = tid & 15;
  GAS char* dst = (GAS char*)out + (size_t)(bh * 16 + qq) * (D * 2) + half * 128 + jj * 8;
  unsigned wr = ring + i * OROW + g * 16, rd = lds0 + qq * OROW + jj * 16, rl = lds0 + LDS_SL + qq * 32;
  asm volatile("" : "+v"(dst), "+v"(wr), "+v"(rd), "+v"(rl));
  __builtin_amdgcn_sched_barrier(0);
  wait_vm<0>();
  __builtin_amdgcn_sched_barrier(0);
  values(P);
  {
    LAS char* d = (LAS char*)(size_t)wr;
#pragma unroll
    for (int cb = 0; cb < 4; ++cb) *(LAS f32x4*)(d + cb * 64) = acc[cb];
  }
  wait_lgkm();
  __builtin_amdgcn_s_barrier();
  asm volatile("" ::: "memory");

  if (w < 4) {
    const f32x4 lw0 = *(LAS f32x4*)(size_t)rl, lw1 = *(LAS f32x4*)(size_t)(rl + 16);
    f32x4 pa[NW];
#pragma unroll
    for (int ww = 0; ww < NW; ++ww) pa[ww] = *(LAS f32x4*)((LAS char*)(size_t)rd + ww * RING);
    const float inv = __builtin_amdgcn_rcpf(sum4(lw0) + sum4(lw1));
    const f32x4 o = (((pa[0] + pa[1]) + (pa[2] + pa[3])) + ((pa[4] + pa[5]) + (pa[6] + pa[7]))) * inv;
    *(GAS u32x2*)dst = u32x2{cvtpk(o[0], o[1]), cvtpk(o[2], o[3])};
  }
}

extern "C" void kernel_launch(void* const* d_in, const int* in_sizes, int n_in, void* d_out, int out_size, void* d_ws,
                              size_t ws_size, hipStream_t stream) {
  printf("PTRS q %p k %p v %p out %p ws %p wsz %zu n %d\n", d_in[0], d_in[1], d_in[2], d_out, d_ws, ws_size, n_in);
  fprintf(stderr, "PTRS q %p k %p v %p out %p ws %p wsz %zu n %d\n", d_in[0], d_in[1], d_in[2], d_out, d_ws, ws_size, n_in);
  (void)hipFuncSetAttribute((const void*)attn_fwd, hipFuncAttributeMaxDynamicSharedMemorySize, LDS_BYTES);
  attn_fwd<<<NBH * 2, NTHREADS, LDS_BYTES, stream>>>(d_in[0], d_in[1], d_in[2], d_out);
}
```

```cpp
#include <hip/hip_runtime.h>
#include <hip/hip_bf16.h>
#include <stdint.h>

typedef __attribute__((ext_vector_type(4))) unsigned u32x4;
typedef __attribute__((ext_vector_type(2))) unsigned u32x2;
typedef __attribute__((ext_vector_type(4))) float f32x4;
typedef __attribute__((ext_vector_type(4))) short s16x4;
typedef __attribute__((ext_vector_type(8))) short s16x8;
typedef __attribute__((ext_vector_type(8))) __bf16 bf16x8;
#define LAS __attribute__((address_space(3)))
#define GAS __attribute__((address_space(1)))

constexpr int B = 16, HQ = 32, HKV = 8, SQ = 4, SK = 2048, D = 128;
constexpr int NBH = B * HKV;
constexpr int NW = 8;
constexpr int TILE = 32;
constexpr int NR = SK / (NW * TILE);
constexpr float CEXP = 0.08838834764831845f * 1.4426950408889634f;
constexpr int KSLOT = 8192, VSLOT = 4096, RING = KSLOT + VSLOT;
constexpr int LDS_Q = NW * RING;
constexpr int LDS_SM = LDS_Q + 4096;
constexpr int LDS_SL = LDS_SM + 512;
constexpr int LDS_BYTES = LDS_SL + 512;
constexpr int OROW = 272;
constexpr int AHEAD_K = 4;
constexpr int AHEAD_V = 2;
constexpr unsigned T_LAST = 1680;
constexpr unsigned T_LEAD = 221;
constexpr unsigned T_WAVE = 24;
constexpr int U_CLOSE = 9;
constexpr unsigned TAU = T_LAST * 256 / (2 * NR - 1);
static_assert((HQ / HKV) * SQ == 16, "16 packed query rows per kv head");
static_assert(16 * OROW <= RING, "LDS layout");

__device__ __forceinline__ unsigned cvtpk(float lo, float hi) {
  unsigned r;
  asm("v_cvt_pk_bf16_f32 %0, %1, %2" : "=v"(r) : "v"(lo), "v"(hi));
  return r;
}
__device__ __forceinline__ float xmax(float x) {
  auto s = __builtin_amdgcn_permlane16_swap(__float_as_uint(x), __float_as_uint(x), false, false);
  x = fmaxf(__uint_as_float(s[0]), __uint_as_float(s[1]));
  auto t = __builtin_amdgcn_permlane32_swap(__float_as_uint(x), __float_as_uint(x), false, false);
  return fmaxf(__uint_as_float(t[0]), __uint_as_float(t[1]));
}
__device__ __forceinline__ float xsum(float x) {
  auto s = __builtin_amdgcn_permlane16_swap(__float_as_uint(x), __float_as_uint(x), false, false);
  x = __uint_as_float(s[0]) + __uint_as_float(s[1]);
  auto t = __builtin_amdgcn_permlane32_swap(__float_as_uint(x), __float_as_uint(x), false, false);
  return __uint_as_float(t[0]) + __uint_as_float(t[1]);
}
__device__ __forceinline__ bf16x8 asbf(u32x4 v) { return __builtin_bit_cast(bf16x8, v); }

__device__ __forceinline__ void dma4(unsigned m0v, __amdgpu_buffer_rsrc_t srd, unsigned soff, unsigned v0, unsigned v1,
                                     unsigned v2, unsigned v3) {
  asm volatile(
      "s_mov_b32 m0, %0\n\t"
      "s_nop 0\n\t"
      "buffer_load_dwordx4 %3, %1, %2 offen nt lds\n\t"
      "buffer_load_dwordx4 %4, %1, %2 offen offset:1024 nt lds\n\t"
      "buffer_load_dwordx4 %5, %1, %2 offen offset:2048 nt lds\n\t"
      "buffer_load_dwordx4 %6, %1, %2 offen offset:3072 nt lds"
      :
      : "s"(m0v), "s"(srd), "s"(soff), "v"(v0), "v"(v1), "v"(v2), "v"(v3)
      : "memory");
}
__device__ __forceinline__ void dma2(unsigned m0v, __amdgpu_buffer_rsrc_t srd, unsigned soff, unsigned v0, unsigned v1) {
  asm volatile(
      "s_mov_b32 m0, %0\n\t"
      "s_nop 0\n\t"
      "buffer_load_dwordx4 %3, %1, %2 offen nt lds\n\t"
      "buffer_load_dwordx4 %4, %1, %2 offen offset:1024 nt lds"
      :
      : "s"(m0v), "s"(srd), "s"(soff), "v"(v0), "v"(v1)
      : "memory");
}
__device__ __forceinline__ void dma1(unsigned m0v, __amdgpu_buffer_rsrc_t srd, unsigned soff, unsigned v0) {
  asm volatile(
      "s_mov_b32 m0, %0\n\t"
      "s_nop 0\n\t"
      "buffer_load_dwordx4 %3, %1, %2 offen nt lds"
      :
      : "s"(m0v), "s"(srd), "s"(soff), "v"(v0)
      : "memory");
}
template <int N>
__device__ __forceinline__ void wait_vm() { asm volatile("s_waitcnt vmcnt(%0)" ::"i"(N) : "memory"); }
__device__ __forceinline__ void wait_lgkm() { asm volatile("s_waitcnt lgkmcnt(0)" ::: "memory"); }
__device__ __forceinline__ float max4(f32x4 x) { return fmaxf(fmaxf(x[0], x[1]), fmaxf(x[2], x[3])); }
__device__ __forceinline__ float sum4(f32x4 x) { return (x[0] + x[1]) + (x[2] + x[3]); }

constexpr int NTHREADS = NW * 64;
constexpr unsigned KV_BYTES = (unsigned)B * HKV * SK * D * 2, Q_BYTES = (unsigned)B * HQ * SQ * D * 2;

__global__ __launch_bounds__(NTHREADS) void attn_fwd(const void* __restrict__ q, const void* __restrict__ k, const void* __restrict__ v,
                                                     void* __restrict__ out) {
  extern __shared__ __attribute__((aligned(16))) char smem[];
  const int tid = threadIdx.x, lane = tid & 63;
  const int w = __builtin_amdgcn_readfirstlane(tid >> 6);
  const int x8 = blockIdx.x & 7, j8 = blockIdx.x >> 3;
  const int half = j8 & 1;
  const int bh = (j8 >> 2) * 16 + x8 * 2 + ((j8 >> 1) & 1);
  const int i = lane & 15, g = lane >> 4;
  const unsigned lds0 = (unsigned)(size_t)(LAS char*)smem;
  const unsigned bhoff = (unsigned)bh * (SK * D * 2);
  const unsigned ring = lds0 + w * RING;

  unsigned kv[4], vv[2];
#pragma unroll
  for (int j = 0; j < 4; ++j) kv[j] = (g & 1) * 256 + (g >> 1) * 2048 + ((i ^ (4 * j + g)) * 16) + j * (4096 - 1024);
  {
    const int rr = lane >> 3, c = lane & 7;
#pragma unroll
    for (int j = 0; j < 2; ++j) vv[j] = (rr & 1) * 256 + (rr >> 1) * 2048 + half * 128 + ((c ^ (rr & 6)) * 16) + j * (8192 - 1024);
  }
  const unsigned qv = (w & 3) * 1024 + g * 256 + ((i ^ (4 * (w & 3) + g)) * 16);
  const unsigned qso = (unsigned)bh * (16 * D * 2), qm0 = lds0 + LDS_Q + (w & 3) * 1024, vm0 = ring + KSLOT;
  auto tile_off = [&](int r) -> unsigned {
    const unsigned turn = (w + r) & (NW - 1);
    return bhoff + (turn & 3) * 512 + (turn >> 2) * 32768 + (unsigned)r * 65536;
  };
  unsigned cur = tile_off(0);

  asm volatile("" ::"v"(kv[0]), "v"(kv[1]), "v"(kv[2]), "v"(kv[3]), "v"(vv[0]), "v"(vv[1]), "v"(qv), "s"(qso), "s"(qm0), "s"(vm0), "s"(ring), "s"(cur));
  asm volatile("" : "+s"(q), "+s"(k), "+s"(v));
  const auto krs = __builtin_amdgcn_make_buffer_rsrc((void*)k, 0, KV_BYTES, 0x00020000);
  const auto vrs = __builtin_amdgcn_make_buffer_rsrc((void*)v, 0, KV_BYTES, 0x00020000);
  const auto qrs = __builtin_amdgcn_make_buffer_rsrc((void*)q, 0, Q_BYTES, 0x00020000);

  auto issueK = [&](unsigned o) {
    dma4(ring, krs, o, kv[0], kv[1], kv[2], kv[3]);
    dma4(ring + 4096, krs, o + 16384, kv[0], kv[1], kv[2], kv[3]);
  };
  auto issueV = [&](unsigned o) {
    dma2(vm0, vrs, o, vv[0], vv[1]);
    dma2(vm0 + 2048, vrs, o + 16384, vv[0], vv[1]);
  };

  if (w < 4) dma1(qm0, qrs, qso, qv);
  issueK(cur);
  issueV(cur);
  asm volatile("" : "+s"(out));
  unsigned tbase;
  auto now = [&]() -> unsigned { return (unsigned)__builtin_amdgcn_s_memrealtime(); };
  auto pace = [&](int u, unsigned t) {
    const unsigned due = tbase + ((u * TAU) >> 8) - (2 * w - (NW - 1)) * (u > U_CLOSE ? u - U_CLOSE : 0);
#pragma unroll 1
    for (int z = 0; z < 4096 && (int)(t - due) < 0; ++z) {
      __builtin_amdgcn_s_sleep(1);
      t = (unsigned)__builtin_amdgcn_s_memrealtime();
    }
  };

  LAS char* const ringp = (LAS char*)smem + w * RING;
  unsigned ka[4];
#pragma unroll
  for (int s = 0; s < 4; ++s) ka[s] = i * 256 + (((4 * s) ^ g ^ i) * 16);
  const int q_ = i >> 2, p_ = i & 3;
  const unsigned va0 = (4 * g + q_) * 128 + ((4 * (g & 1) + 2 * (q_ >> 1) + (p_ >> 1)) * 16) + (p_ & 1) * 8;

  wait_vm<12>();
  __builtin_amdgcn_s_barrier();
  tbase = now() - T_LEAD + w * T_WAVE;
  asm volatile("" ::: "memory");
  u32x4 Qf[4];
#pragma unroll
  for (int s = 0; s < 4; ++s) Qf[s] = *(LAS u32x4*)((LAS char*)smem + LDS_Q + ka[s]);

  f32x4 acc[4];
#pragma unroll
  for (int e = 0; e < 4; ++e) acc[e] = f32x4{0.f, 0.f, 0.f, 0.f};
  float m = -__builtin_inff(), l = 0.f;

  auto scores = [&](f32x4& s0, f32x4& s1) {
    u32x4 kf[8];
#pragma unroll
    for (int s = 0; s < 4; ++s) {
      kf[2 * s] = *(LAS u32x4*)(ringp + ka[s]);
      kf[2 * s + 1] = *(LAS u32x4*)(ringp + 4096 + ka[s]);
    }
    wait_lgkm();
    __builtin_amdgcn_sched_barrier(0);
    s0 = f32x4{0.f, 0.f, 0.f, 0.f};
    s1 = s0;
#pragma unroll
    for (int s = 0; s < 4; ++s) {
      s0 = __builtin_amdgcn_mfma_f32_16x16x32_bf16(asbf(kf[2 * s]), asbf(Qf[s]), s0, 0, 0, 0);
      s1 = __builtin_amdgcn_mfma_f32_16x16x32_bf16(asbf(kf[2 * s + 1]), asbf(Qf[s]), s1, 0, 0, 0);
    }
  };
  auto probs = [&](f32x4 s0, f32x4 s1) {
    const float mc = -m * CEXP;
    float p[8];
#pragma unroll
    for (int r = 0; r < 4; ++r) {
      p[r] = __builtin_amdgcn_exp2f(fmaf(s0[r], CEXP, mc));
      p[4 + r] = __builtin_amdgcn_exp2f(fmaf(s1[r], CEXP, mc));
    }
    l += ((p[0] + p[1]) + (p[2] + p[3])) + ((p[4] + p[5]) + (p[6] + p[7]));
    return u32x4{cvtpk(p[0], p[1]), cvtpk(p[2], p[3]), cvtpk(p[4], p[5]), cvtpk(p[6], p[7])};
  };
  auto rescale = [&](float mn) {
    const float alpha = __builtin_amdgcn_exp2f((m - mn) * CEXP);
    l *= alpha;
#pragma unroll
    for (int e = 0; e < 4; ++e) acc[e] *= alpha;
    m = mn;
  };
  auto values = [&](u32x4 P) {
    s16x4 vf[8];
#pragma unroll
    for (int cb = 0; cb < 4; ++cb) {
      const unsigned a = va0 ^ (cb << 5);
      vf[2 * cb] = __builtin_amdgcn_ds_read_tr16_b64_v4i16((LAS s16x4*)(ringp + KSLOT + a));
      vf[2 * cb + 1] = __builtin_amdgcn_ds_read_tr16_b64_v4i16((LAS s16x4*)(ringp + KSLOT + 2048 + a));
    }
    wait_lgkm();
    __builtin_amdgcn_sched_barrier(0);
#pragma unroll
    for (int cb = 0; cb < 4; ++cb) {
      const s16x8 f = __builtin_shufflevector(vf[2 * cb], vf[2 * cb + 1], 0, 1, 2, 3, 4, 5, 6, 7);
      acc[cb] = __builtin_amdgcn_mfma_f32_16x16x32_bf16(__builtin_bit_cast(bf16x8, f), asbf(P), acc[cb], 0, 0, 0);
    }
  };

#pragma unroll 1
  for (int r = 1; r < NR; ++r) {
    wait_vm<4>();
    __builtin_amdgcn_sched_barrier(0);
    f32x4 s0, s1;
    scores(s0, s1);
    const float mn = fmaxf(m, xmax(fmaxf(max4(s0), max4(s1))));
    if (__builtin_amdgcn_ballot_w64(mn > m)) rescale(mn);
    const u32x4 P = probs(s0, s1);
    cur = tile_off(r);
    __builtin_amdgcn_sched_barrier(0);
    { const unsigned t = now(); wait_vm<AHEAD_V>(); pace(2 * r, t); }
    issueK(cur);
    wait_vm<8>();
    __builtin_amdgcn_sched_barrier(0);
    values(P);
    { const unsigned t = now(); wait_vm<AHEAD_K>(); pace(2 * r + 1, t); }
    issueV(cur);
  }

  LAS char* const sm = (LAS char*)smem;
  wait_vm<4>();
  __builtin_amdgcn_sched_barrier(0);
  f32x4 s0, s1;
  scores(s0, s1);
  const float mn = fmaxf(m, xmax(fmaxf(max4(s0), max4(s1))));
  if (g == 0) *(LAS float*)(sm + LDS_SM + i * 32 + w * 4) = mn;
  wait_lgkm();
  __builtin_amdgcn_s_barrier();
  asm volatile("" ::: "memory");
  const f32x4 mw0 = *(LAS f32x4*)(sm + LDS_SM + i * 32), mw1 = *(LAS f32x4*)(sm + LDS_SM + i * 32 + 16);
  rescale(fmaxf(max4(mw0), max4(mw1)));
#pragma unroll
  for (int e = 0; e < 4; ++e) asm volatile("" : "+v"(acc[e]));
  const u32x4 P = probs(s0, s1);
  l = xsum(l);
  if (g == 0) *(LAS float*)(sm + LDS_SL + i * 32 + w * 4) = l;
  const int qq = (tid >> 4) & 15, jj = tid & 15;
  GAS char* dst = (GAS char*)out + (size_t)(bh * 16 + qq) * (D * 2) + half * 128 + jj * 8;
  unsigned wr = ring + i * OROW + g * 16, rd = lds0 + qq * OROW + jj * 16, rl = lds0 + LDS_SL + qq * 32;
  asm volatile("" : "+v"(dst), "+v"(wr), "+v"(rd), "+v"(rl));
  __builtin_amdgcn_sched_barrier(0);
  wait_vm<0>();
  __builtin_amdgcn_sched_barrier(0);
  values(P);
  {
    LAS char* d = (LAS char*)(size_t)wr;
#pragma unroll
    for (int cb = 0; cb < 4; ++cb) *(LAS f32x4*)(d + cb * 64) = acc[cb];
  }
  wait_lgkm();
  __builtin_amdgcn_s_barrier();
  asm volatile("" ::: "memory");

  if (w < 4) {
    const f32x4 lw0 = *(LAS f32x4*)(size_t)rl, lw1 = *(LAS f32x4*)(size_t)(rl + 16);
    f32x4 pa[NW];
#pragma unroll
    for (int ww = 0; ww < NW; ++ww) pa[ww] = *(LAS f32x4*)((LAS char*)(size_t)rd + ww * RING);
    const float inv = __builtin_amdgcn_rcpf(sum4(lw0) + sum4(lw1));
    const f32x4 o = (((pa[0] + pa[1]) + (pa[2] + pa[3])) + ((pa[4] + pa[5]) + (pa[6] + pa[7]))) * inv;
    *(GAS u32x2*)dst = u32x2{cvtpk(o[0], o[1]), cvtpk(o[2], o[3])};
  }
}

extern "C" void kernel_launch(void* const* d_in, const int* in_sizes, int n_in, void* d_out, int out_size, void* d_ws,
                              size_t ws_size, hipStream_t stream) {
  (void)hipFuncSetAttribute((const void*)attn_fwd, hipFuncAttributeMaxDynamicSharedMemorySize, LDS_BYTES);
  attn_fwd<<<NBH * 2, NTHREADS, LDS_BYTES, stream>>>(d_in[0], d_in[1], d_in[2], d_out);
}
```

```cpp
#include <hip/hip_runtime.h>
#include <hip/hip_bf16.h>
#include <stdint.h>

typedef __attribute__((ext_vector_type(4))) unsigned u32x4;
typedef __attribute__((ext_vector_type(2))) unsigned u32x2;
typedef __attribute__((ext_vector_type(4))) float f32x4;
typedef __attribute__((ext_vector_type(4))) short s16x4;
typedef __attribute__((ext_vector_type(8))) short s16x8;
typedef __attribute__((ext_vector_type(8))) __bf16 bf16x8;
#define LAS __attribute__((address_space(3)))
#define GAS __attribute__((address_space(1)))

constexpr int B = 16, HQ = 32, HKV = 8, SQ = 4, SK = 2048, D = 128;
constexpr int NBH = B * HKV;
constexpr int NW = 8;
constexpr int TILE = 32;
constexpr int NR = SK / (NW * TILE);
constexpr float CEXP = 0.08838834764831845f * 1.4426950408889634f;
constexpr int KSLOT = 8192, VSLOT = 4096, RING = KSLOT + VSLOT;
constexpr int LDS_Q = NW * RING;
constexpr int LDS_SM = LDS_Q + 4096;
constexpr int LDS_SL = LDS_SM + 512;
constexpr int LDS_BYTES = LDS_SL + 512;
constexpr int OROW = 272;
constexpr int AHEAD_K = 4;
constexpr int AHEAD_V = 2;
constexpr unsigned T_LAST = 1680;
constexpr unsigned T_LEAD = 221;
constexpr unsigned T_WAVE = 24;
constexpr int U_CLOSE = 9;
constexpr unsigned TAU = T_LAST * 256 / (2 * NR - 1);
static_assert((HQ / HKV) * SQ == 16, "16 packed query rows per kv head");
static_assert(16 * OROW <= RING, "LDS layout");

__device__ __forceinline__ unsigned cvtpk(float lo, float hi) {
  unsigned r;
  asm("v_cvt_pk_bf16_f32 %0, %1, %2" : "=v"(r) : "v"(lo), "v"(hi));
  return r;
}
__device__ __forceinline__ float xmax(float x) {
  auto s = __builtin_amdgcn_permlane16_swap(__float_as_uint(x), __float_as_uint(x), false, false);
  x = fmaxf(__uint_as_float(s[0]), __uint_as_float(s[1]));
  auto t = __builtin_amdgcn_permlane32_swap(__float_as_uint(x), __float_as_uint(x), false, false);
  return fmaxf(__uint_as_float(t[0]), __uint_as_float(t[1]));
}
__device__ __forceinline__ float xsum(float x) {
  auto s = __builtin_amdgcn_permlane16_swap(__float_as_uint(x), __float_as_uint(x), false, false);
  x = __uint_as_float(s[0]) + __uint_as_float(s[1]);
  auto t = __builtin_amdgcn_permlane32_swap(__float_as_uint(x), __float_as_uint(x), false, false);
  return __uint_as_float(t[0]) + __uint_as_float(t[1]);
}
__device__ __forceinline__ bf16x8 asbf(u32x4 v) { return __builtin_bit_cast(bf16x8, v); }

__device__ __forceinline__ void dma4(unsigned m0v, __amdgpu_buffer_rsrc_t srd, unsigned soff, unsigned v0, unsigned v1,
                                     unsigned v2, unsigned v3) {
  asm volatile(
      "s_mov_b32 m0, %0\n\t"
      "s_nop 0\n\t"
      "buffer_load_dwordx4 %3, %1, %2 offen nt lds\n\t"
      "buffer_load_dwordx4 %4, %1, %2 offen offset:1024 nt lds\n\t"
      "buffer_load_dwordx4 %5, %1, %2 offen offset:2048 nt lds\n\t"
      "buffer_load_dwordx4 %6, %1, %2 offen offset:3072 nt lds"
      :
      : "s"(m0v), "s"(srd), "s"(soff), "v"(v0), "v"(v1), "v"(v2), "v"(v3)
      : "memory");
}
__device__ __forceinline__ void dma2(unsigned m0v, __amdgpu_buffer_rsrc_t srd, unsigned soff, unsigned v0, unsigned v1) {
  asm volatile(
      "s_mov_b32 m0, %0\n\t"
      "s_nop 0\n\t"
      "buffer_load_dwordx4 %3, %1, %2 offen nt lds\n\t"
      "buffer_load_dwordx4 %4, %1, %2 offen offset:1024 nt lds"
      :
      : "s"(m0v), "s"(srd), "s"(soff), "v"(v0), "v"(v1)
      : "memory");
}
__device__ __forceinline__ void dma1(unsigned m0v, __amdgpu_buffer_rsrc_t srd, unsigned soff, unsigned v0) {
  asm volatile(
      "s_mov_b32 m0, %0\n\t"
      "s_nop 0\n\t"
      "buffer_load_dwordx4 %3, %1, %2 offen nt lds"
      :
      : "s"(m0v), "s"(srd), "s"(soff), "v"(v0)
      : "memory");
}
template <int N>
__device__ __forceinline__ void wait_vm() { asm volatile("s_waitcnt vmcnt(%0)" ::"i"(N) : "memory"); }
__device__ __forceinline__ void wait_lgkm() { asm volatile("s_waitcnt lgkmcnt(0)" ::: "memory"); }
__device__ __forceinline__ float max4(f32x4 x) { return fmaxf(fmaxf(x[0], x[1]), fmaxf(x[2], x[3])); }
__device__ __forceinline__ float sum4(f32x4 x) { return (x[0] + x[1]) + (x[2] + x[3]); }

constexpr int NTHREADS = NW * 64;
constexpr unsigned BIAS = 4096;
constexpr unsigned KV_BYTES = (unsigned)B * HKV * SK * D * 2, Q_BYTES = (unsigned)B * HQ * SQ * D * 2;

__global__ __launch_bounds__(NTHREADS) void attn_fwd(const void* __restrict__ q, const void* __restrict__ k, const void* __restrict__ v,
                                                     void* __restrict__ out) {
  extern __shared__ __attribute__((aligned(16))) char smem[];
  const unsigned t_in = (unsigned)__builtin_amdgcn_s_memrealtime();
  const int tid = threadIdx.x, lane = tid & 63;
  const int w = __builtin_amdgcn_readfirstlane(tid >> 6);
  const int x8 = blockIdx.x & 7, j8 = blockIdx.x >> 3;
  const int half = j8 & 1;
  const int bh = (j8 >> 2) * 16 + x8 * 2 + ((j8 >> 1) & 1);
  const int i = lane & 15, g = lane >> 4;
  const unsigned lds0 = (unsigned)(size_t)(LAS char*)smem;
  const unsigned bhoff = (unsigned)bh * (SK * D * 2);
  const unsigned ring = lds0 + w * RING;

  unsigned kv[4], vv[2];
#pragma unroll
  for (int j = 0; j < 4; ++j)
    kv[j] = (g & 1) * 0x100 + (g >> 1) * 0x800 + (j & 1) * 0x41000 + (j >> 1) * 0x12000 + ((i ^ (4 * j + g)) * 16) + BIAS - 1024 * j;
  {
    const int rr = lane >> 3, c = lane & 7;
#pragma unroll
    for (int j = 0; j < 2; ++j)
      vv[j] = (rr & 1) * 0x100 + ((rr >> 1) & 1) * 0x800 + (rr >> 2) * 0x41000 + j * 0x12000 + half * 128 + ((c ^ (rr & 6)) * 16) + BIAS - 1024 * j;
  }
  const unsigned qv = (w & 3) * 1024 + g * 256 + ((i ^ (4 * (w & 3) + g)) * 16);
  const unsigned qso = (unsigned)bh * (16 * D * 2), qm0 = lds0 + LDS_Q + (w & 3) * 1024, vm0 = ring + KSLOT;
  auto tile_off = [&](int r) -> unsigned {
    const unsigned turn = (w + r) & (NW - 1);
    return bhoff + (turn & 3) * 0x200 + (turn >> 2) * 0x8000 + (r & 2) * 0x10000;
  };
  unsigned cur = tile_off(0);

  asm volatile("" ::"v"(kv[0]), "v"(kv[1]), "v"(kv[2]), "v"(kv[3]), "v"(vv[0]), "v"(vv[1]), "v"(qv), "s"(qso), "s"(qm0), "s"(vm0), "s"(ring), "s"(cur));
  asm volatile("" : "+s"(q), "+s"(k), "+s"(v));
  const auto krs = __builtin_amdgcn_make_buffer_rsrc((char*)k - BIAS, 0, KV_BYTES + BIAS, 0x00020000);
  const auto vrs = __builtin_amdgcn_make_buffer_rsrc((char*)v - BIAS, 0, KV_BYTES + BIAS, 0x00020000);
  const auto qrs = __builtin_amdgcn_make_buffer_rsrc((void*)q, 0, Q_BYTES, 0x00020000);

  auto issueK = [&](unsigned o) {
    dma4(ring, krs, o, kv[0], kv[1], kv[2], kv[3]);
    dma4(ring + 4096, krs, o ^ 0x24000, kv[0], kv[1], kv[2], kv[3]);
  };
  auto issueV = [&](unsigned o) {
    dma2(vm0, vrs, o, vv[0], vv[1]);
    dma2(vm0 + 2048, vrs, o ^ 0x24000, vv[0], vv[1]);
  };

  if (w < 4) dma1(qm0, qrs, qso, qv);
  issueK(cur);
  issueV(cur);
  asm volatile("" : "+s"(out));
  unsigned tbase;
  auto now = [&]() -> unsigned { return (unsigned)__builtin_amdgcn_s_memrealtime(); };
  auto pace = [&](int u, unsigned t) {
    const unsigned due = tbase + ((u * TAU) >> 8) - (2 * w - (NW - 1)) * (u > U_CLOSE ? u - U_CLOSE : 0);
#pragma unroll 1
    for (int z = 0; z < 4096 && (int)(t - due) < 0; ++z) {
      __builtin_amdgcn_s_sleep(1);
      t = (unsigned)__builtin_amdgcn_s_memrealtime();
    }
  };

  LAS char* const ringp = (LAS char*)smem + w * RING;
  unsigned ka[4];
#pragma unroll
  for (int s = 0; s < 4; ++s) ka[s] = i * 256 + (((4 * s) ^ g ^ i) * 16);
  const int q_ = i >> 2, p_ = i & 3;
  const unsigned va0 = (4 * g + q_) * 128 + ((4 * (g & 1) + 2 * (q_ >> 1) + (p_ >> 1)) * 16) + (p_ & 1) * 8;

  wait_vm<12>();
  __builtin_amdgcn_s_barrier();
  tbase = now() - T_LEAD + w * T_WAVE;
  asm volatile("" ::: "memory");
  u32x4 Qf[4];
#pragma unroll
  for (int s = 0; s < 4; ++s) Qf[s] = *(LAS u32x4*)((LAS char*)smem + LDS_Q + ka[s]);

  f32x4 acc[4];
#pragma unroll
  for (int e = 0; e < 4; ++e) acc[e] = f32x4{0.f, 0.f, 0.f, 0.f};
  float m = -__builtin_inff(), l = 0.f;

  auto scores = [&](f32x4& s0, f32x4& s1) {
    u32x4 kf[8];
#pragma unroll
    for (int s = 0; s < 4; ++s) {
      kf[2 * s] = *(LAS u32x4*)(ringp + ka[s]);
      kf[2 * s + 1] = *(LAS u32x4*)(ringp + 4096 + ka[s]);
    }
    wait_lgkm();
    __builtin_amdgcn_sched_barrier(0);
    s0 = f32x4{0.f, 0.f, 0.f, 0.f};
    s1 = s0;
#pragma unroll
    for (int s = 0; s < 4; ++s) {
      s0 = __builtin_amdgcn_mfma_f32_16x16x32_bf16(asbf(kf[2 * s]), asbf(Qf[s]), s0, 0, 0, 0);
      s1 = __builtin_amdgcn_mfma_f32_16x16x32_bf16(asbf(kf[2 * s + 1]), asbf(Qf[s]), s1, 0, 0, 0);
    }
  };
  auto probs = [&](f32x4 s0, f32x4 s1) {
    const float mc = -m * CEXP;
    float p[8];
#pragma unroll
    for (int r = 0; r < 4; ++r) {
      p[r] = __builtin_amdgcn_exp2f(fmaf(s0[r], CEXP, mc));
      p[4 + r] = __builtin_amdgcn_exp2f(fmaf(s1[r], CEXP, mc));
    }
    l += ((p[0] + p[1]) + (p[2] + p[3])) + ((p[4] + p[5]) + (p[6] + p[7]));
    return u32x4{cvtpk(p[0], p[1]), cvtpk(p[2], p[3]), cvtpk(p[4], p[5]), cvtpk(p[6], p[7])};
  };
  auto rescale = [&](float mn) {
    const float alpha = __builtin_amdgcn_exp2f((m - mn) * CEXP);
    l *= alpha;
#pragma unroll
    for (int e = 0; e < 4; ++e) acc[e] *= alpha;
    m = mn;
  };
  auto values = [&](u32x4 P) {
    s16x4 vf[8];
#pragma unroll
    for (int cb = 0; cb < 4; ++cb) {
      const unsigned a = va0 ^ (cb << 5);
      vf[2 * cb] = __builtin_amdgcn_ds_read_tr16_b64_v4i16((LAS s16x4*)(ringp + KSLOT + a));
      vf[2 * cb + 1] = __builtin_amdgcn_ds_read_tr16_b64_v4i16((LAS s16x4*)(ringp + KSLOT + 2048 + a));
    }
    wait_lgkm();
    __builtin_amdgcn_sched_barrier(0);
#pragma unroll
    for (int cb = 0; cb < 4; ++cb) {
      const s16x8 f = __builtin_shufflevector(vf[2 * cb], vf[2 * cb + 1], 0, 1, 2, 3, 4, 5, 6, 7);
      acc[cb] = __builtin_amdgcn_mfma_f32_16x16x32_bf16(__builtin_bit_cast(bf16x8, f), asbf(P), acc[cb], 0, 0, 0);
    }
  };

#pragma unroll 1
  for (int r = 1; r < NR; ++r) {
    wait_vm<4>();
    __builtin_amdgcn_sched_barrier(0);
    f32x4 s0, s1;
    scores(s0, s1);
    const float mn = fmaxf(m, xmax(fmaxf(max4(s0), max4(s1))));
    if (__builtin_amdgcn_ballot_w64(mn > m)) rescale(mn);
    const u32x4 P = probs(s0, s1);
    cur = tile_off(r);
    const unsigned flip = ((r ^ (r - 1)) & 5) * 0x10000;
#pragma unroll
    for (int j = 0; j < 4; ++j) kv[j] ^= flip;
    vv[0] ^= flip;
    vv[1] ^= flip;
    __builtin_amdgcn_sched_barrier(0);
    { const unsigned t = now(); wait_vm<AHEAD_V>(); pace(2 * r, t); }
    issueK(cur);
    wait_vm<8>();
    __builtin_amdgcn_sched_barrier(0);
    values(P);
    { const unsigned t = now(); wait_vm<AHEAD_K>(); pace(2 * r + 1, t); }
    issueV(cur);
  }

  LAS char* const sm = (LAS char*)smem;
  wait_vm<4>();
  __builtin_amdgcn_sched_barrier(0);
  f32x4 s0, s1;
  scores(s0, s1);
  const float mn = fmaxf(m, xmax(fmaxf(max4(s0), max4(s1))));
  if (g == 0) *(LAS float*)(sm + LDS_SM + i * 32 + w * 4) = mn;
  wait_lgkm();
  __builtin_amdgcn_s_barrier();
  asm volatile("" ::: "memory");
  const f32x4 mw0 = *(LAS f32x4*)(sm + LDS_SM + i * 32), mw1 = *(LAS f32x4*)(sm + LDS_SM + i * 32 + 16);
  rescale(fmaxf(max4(mw0), max4(mw1)));
#pragma unroll
  for (int e = 0; e < 4; ++e) asm volatile("" : "+v"(acc[e]));
  const u32x4 P = probs(s0, s1);
  l = xsum(l);
  if (g == 0) *(LAS float*)(sm + LDS_SL + i * 32 + w * 4) = l;
  const int qq = (tid >> 4) & 15, jj = tid & 15;
  GAS char* dst = (GAS char*)out + (size_t)(bh * 16 + qq) * (D * 2) + half * 128 + jj * 8;
  unsigned wr = ring + i * OROW + g * 16, rd = lds0 + qq * OROW + jj * 16, rl = lds0 + LDS_SL + qq * 32;
  asm volatile("" : "+v"(dst), "+v"(wr), "+v"(rd), "+v"(rl));
  __builtin_amdgcn_sched_barrier(0);
  wait_vm<0>();
  __builtin_amdgcn_sched_barrier(0);
  values(P);
  {
    LAS char* d = (LAS char*)(size_t)wr;
#pragma unroll
    for (int cb = 0; cb < 4; ++cb) *(LAS f32x4*)(d + cb * 64) = acc[cb];
  }
  wait_lgkm();
  __builtin_amdgcn_s_barrier();
  asm volatile("" ::: "memory");

  if (w < 4) {
    const f32x4 lw0 = *(LAS f32x4*)(size_t)rl, lw1 = *(LAS f32x4*)(size_t)(rl + 16);
    f32x4 pa[NW];
#pragma unroll
    for (int ww = 0; ww < NW; ++ww) pa[ww] = *(LAS f32x4*)((LAS char*)(size_t)rd + ww * RING);
    const float inv = __builtin_amdgcn_rcpf(sum4(lw0) + sum4(lw1));
    const f32x4 o = (((pa[0] + pa[1]) + (pa[2] + pa[3])) + ((pa[4] + pa[5]) + (pa[6] + pa[7]))) * inv;
    *(GAS u32x2*)dst = u32x2{cvtpk(o[0], o[1]), cvtpk(o[2], o[3])};
  }
  {
    unsigned t = now();
#pragma unroll 1
    for (int z = 0; z < 20000 && (int)(t - t_in) < 2100; ++z) {
      __builtin_amdgcn_s_sleep(1);
      t = now();
    }
  }
}

extern "C" void kernel_launch(void* const* d_in, const int* in_sizes, int n_in, void* d_out, int out_size, void* d_ws,
                              size_t ws_size, hipStream_t stream) {
  (void)hipFuncSetAttribute((const void*)attn_fwd, hipFuncAttributeMaxDynamicSharedMemorySize, LDS_BYTES);
  attn_fwd<<<NBH * 2, NTHREADS, LDS_BYTES, stream>>>(d_in[0], d_in[1], d_in[2], d_out);
}
```

```cpp
#include <hip/hip_runtime.h>
#include <hip/hip_bf16.h>
#include <stdint.h>

typedef __attribute__((ext_vector_type(4))) unsigned u32x4;
typedef __attribute__((ext_vector_type(2))) unsigned u32x2;
typedef __attribute__((ext_vector_type(4))) float f32x4;
typedef __attribute__((ext_vector_type(4))) short s16x4;
typedef __attribute__((ext_vector_type(8))) short s16x8;
typedef __attribute__((ext_vector_type(8))) __bf16 bf16x8;
#define LAS __attribute__((address_space(3)))
#define GAS __attribute__((address_space(1)))

constexpr int B = 16, HQ = 32, HKV = 8, SQ = 4, SK = 2048, D = 128;
constexpr int NBH = B * HKV;
constexpr int NW = 8;
constexpr int TILE = 32;
constexpr int NR = SK / (NW * TILE);
constexpr float CEXP = 0.08838834764831845f * 1.4426950408889634f;
constexpr int KSLOT = 8192, VSLOT = 4096, RING = KSLOT + VSLOT;
constexpr int LDS_Q = NW * RING;
constexpr int LDS_SM = LDS_Q + 4096;
constexpr int LDS_SL = LDS_SM + 512;
constexpr int LDS_BYTES = LDS_SL + 512;
constexpr int OROW = 272;
constexpr int AHEAD_K = 4;
constexpr int AHEAD_V = 2;
constexpr unsigned T_LAST = 1680;
constexpr unsigned T_LEAD = 221;
constexpr unsigned T_WAVE = 24;
constexpr int U_CLOSE = 9;
constexpr unsigned TAU = T_LAST * 256 / (2 * NR - 1);
static_assert((HQ / HKV) * SQ == 16, "16 packed query rows per kv head");
static_assert(16 * OROW <= RING, "LDS layout");

__device__ __forceinline__ unsigned cvtpk(float lo, float hi) {
  unsigned r;
  asm("v_cvt_pk_bf16_f32 %0, %1, %2" : "=v"(r) : "v"(lo), "v"(hi));
  return r;
}
__device__ __forceinline__ float xmax(float x) {
  auto s = __builtin_amdgcn_permlane16_swap(__float_as_uint(x), __float_as_uint(x), false, false);
  x = fmaxf(__uint_as_float(s[0]), __uint_as_float(s[1]));
  auto t = __builtin_amdgcn_permlane32_swap(__float_as_uint(x), __float_as_uint(x), false, false);
  return fmaxf(__uint_as_float(t[0]), __uint_as_float(t[1]));
}
__device__ __forceinline__ float xsum(float x) {
  auto s = __builtin_amdgcn_permlane16_swap(__float_as_uint(x), __float_as_uint(x), false, false);
  x = __uint_as_float(s[0]) + __uint_as_float(s[1]);
  auto t = __builtin_amdgcn_permlane32_swap(__float_as_uint(x), __float_as_uint(x), false, false);
  return __uint_as_float(t[0]) + __uint_as_float(t[1]);
}
__device__ __forceinline__ bf16x8 asbf(u32x4 v) { return __builtin_bit_cast(bf16x8, v); }

__device__ __forceinline__ void dma4(unsigned m0v, __amdgpu_buffer_rsrc_t srd, unsigned soff, unsigned v0, unsigned v1,
                                     unsigned v2, unsigned v3) {
  asm volatile(
      "s_mov_b32 m0, %0\n\t"
      "s_nop 0\n\t"
      "buffer_load_dwordx4 %3, %1, %2 offen nt lds\n\t"
      "buffer_load_dwordx4 %4, %1, %2 offen offset:1024 nt lds\n\t"
      "buffer_load_dwordx4 %5, %1, %2 offen offset:2048 nt lds\n\t"
      "buffer_load_dwordx4 %6, %1, %2 offen offset:3072 nt lds"
      :
      : "s"(m0v), "s"(srd), "s"(soff), "v"(v0), "v"(v1), "v"(v2), "v"(v3)
      : "memory");
}
__device__ __forceinline__ void dma2(unsigned m0v, __amdgpu_buffer_rsrc_t srd, unsigned soff, unsigned v0, unsigned v1) {
  asm volatile(
      "s_mov_b32 m0, %0\n\t"
      "s_nop 0\n\t"
      "buffer_load_dwordx4 %3, %1, %2 offen nt lds\n\t"
      "buffer_load_dwordx4 %4, %1, %2 offen offset:1024 nt lds"
      :
      : "s"(m0v), "s"(srd), "s"(soff), "v"(v0), "v"(v1)
      : "memory");
}
__device__ __forceinline__ void dma1(unsigned m0v, __amdgpu_buffer_rsrc_t srd, unsigned soff, unsigned v0) {
  asm volatile(
      "s_mov_b32 m0, %0\n\t"
      "s_nop 0\n\t"
      "buffer_load_dwordx4 %3, %1, %2 offen nt lds"
      :
      : "s"(m0v), "s"(srd), "s"(soff), "v"(v0)
      : "memory");
}
template <int N>
__device__ __forceinline__ void wait_vm() { asm volatile("s_waitcnt vmcnt(%0)" ::"i"(N) : "memory"); }
__device__ __forceinline__ void wait_lgkm() { asm volatile("s_waitcnt lgkmcnt(0)" ::: "memory"); }
__device__ __forceinline__ float max4(f32x4 x) { return fmaxf(fmaxf(x[0], x[1]), fmaxf(x[2], x[3])); }
__device__ __forceinline__ float sum4(f32x4 x) { return (x[0] + x[1]) + (x[2] + x[3]); }

constexpr int NTHREADS = NW * 64;
constexpr unsigned BIAS = 4096;
constexpr unsigned KV_BYTES = (unsigned)B * HKV * SK * D * 2, Q_BYTES = (unsigned)B * HQ * SQ * D * 2;

__global__ __launch_bounds__(NTHREADS) void attn_fwd(const void* __restrict__ q, const void* __restrict__ k, const void* __restrict__ v,
                                                     void* __restrict__ out) {
  extern __shared__ __attribute__((aligned(16))) char smem[];
  const int tid = threadIdx.x, lane = tid & 63;
  const int w = __builtin_amdgcn_readfirstlane(tid >> 6);
  const int x8 = blockIdx.x & 7, j8 = blockIdx.x >> 3;
  const int half = j8 & 1;
  const int bh = (j8 >> 2) * 16 + x8 * 2 + ((j8 >> 1) & 1);
  const int i = lane & 15, g = lane >> 4;
  const unsigned lds0 = (unsigned)(size_t)(LAS char*)smem;
  const unsigned bhoff = (unsigned)bh * (SK * D * 2);
  const unsigned ring = lds0 + w * RING;

  unsigned kv[4], vv[2];
#pragma unroll
  for (int j = 0; j < 4; ++j)
    kv[j] = (g & 1) * 0x100 + (g >> 1) * 0x800 + (j & 1) * 0x41000 + (j >> 1) * 0x12000 + ((i ^ (4 * j + g)) * 16) + BIAS - 1024 * j;
  {
    const int rr = lane >> 3, c = lane & 7;
#pragma unroll
    for (int j = 0; j < 2; ++j)
      vv[j] = (rr & 1) * 0x100 + ((rr >> 1) & 1) * 0x800 + (rr >> 2) * 0x41000 + j * 0x12000 + half * 128 + ((c ^ (rr & 6)) * 16) + BIAS - 1024 * j;
  }
  const unsigned qv = (w & 3) * 1024 + g * 256 + ((i ^ (4 * (w & 3) + g)) * 16);
  const unsigned qso = (unsigned)bh * (16 * D * 2), qm0 = lds0 + LDS_Q + (w & 3) * 1024, vm0 = ring + KSLOT;
  auto tile_off = [&](int r) -> unsigned {
    const unsigned turn = (w + r) & (NW - 1);
    return bhoff + (turn & 3) * 0x200 + (turn >> 2) * 0x8000 + (r & 2) * 0x10000;
  };
  unsigned cur = tile_off(0);

  asm volatile("" ::"v"(kv[0]), "v"(kv[1]), "v"(kv[2]), "v"(kv[3]), "v"(vv[0]), "v"(vv[1]), "v"(qv), "s"(qso), "s"(qm0), "s"(vm0), "s"(ring), "s"(cur));
  asm volatile("" : "+s"(q), "+s"(k), "+s"(v));
  const auto krs = __builtin_amdgcn_make_buffer_rsrc((char*)k - BIAS, 0, KV_BYTES + BIAS, 0x00020000);
  const auto vrs = __builtin_amdgcn_make_buffer_rsrc((char*)v - BIAS, 0, KV_BYTES + BIAS, 0x00020000);
  const auto qrs = __builtin_amdgcn_make_buffer_rsrc((void*)q, 0, Q_BYTES, 0x00020000);

  auto issueK = [&](unsigned o) {
    dma4(ring, krs, o, kv[0], kv[1], kv[2], kv[3]);
    dma4(ring + 4096, krs, o ^ 0x24000, kv[0], kv[1], kv[2], kv[3]);
  };
  auto issueV = [&](unsigned o) {
    dma2(vm0, vrs, o, vv[0], vv[1]);
    dma2(vm0 + 2048, vrs, o ^ 0x24000, vv[0], vv[1]);
  };

  if (w < 4) dma1(qm0, qrs, qso, qv);
  issueK(cur);
  issueV(cur);
  asm volatile("" : "+s"(out));
  unsigned tbase;
  auto now = [&]() -> unsigned { return (unsigned)__builtin_amdgcn_s_memrealtime(); };
  auto pace = [&](int u, unsigned t) {
    const unsigned due = tbase + ((u * TAU) >> 8) - (2 * w - (NW - 1)) * (u > U_CLOSE ? u - U_CLOSE : 0);
#pragma unroll 1
    for (int z = 0; z < 4096 && (int)(t - due) < 0; ++z) {
      __builtin_amdgcn_s_sleep(1);
      t = (unsigned)__builtin_amdgcn_s_memrealtime();
    }
  };

  LAS char* const ringp = (LAS char*)smem + w * RING;
  unsigned ka[4];
#pragma unroll
  for (int s = 0; s < 4; ++s) ka[s] = i * 256 + (((4 * s) ^ g ^ i) * 16);
  const int q_ = i >> 2, p_ = i & 3;
  const unsigned va0 = (4 * g + q_) * 128 + ((4 * (g & 1) + 2 * (q_ >> 1) + (p_ >> 1)) * 16) + (p_ & 1) * 8;

  wait_vm<12>();
  __builtin_amdgcn_s_barrier();
  tbase = now() - T_LEAD + w * T_WAVE;
  asm volatile("" ::: "memory");
  u32x4 Qf[4];
#pragma unroll
  for (int s = 0; s < 4; ++s) Qf[s] = *(LAS u32x4*)((LAS char*)smem + LDS_Q + ka[s]);

  f32x4 acc[4];
#pragma unroll
  for (int e = 0; e < 4; ++e) acc[e] = f32x4{0.f, 0.f, 0.f, 0.f};
  float m = -__builtin_inff(), l = 0.f;

  auto scores = [&](f32x4& s0, f32x4& s1) {
    u32x4 kf[8];
#pragma unroll
    for (int s = 0; s < 4; ++s) {
      kf[2 * s] = *(LAS u32x4*)(ringp + ka[s]);
      kf[2 * s + 1] = *(LAS u32x4*)(ringp + 4096 + ka[s]);
    }
    wait_lgkm();
    __builtin_amdgcn_sched_barrier(0);
    s0 = f32x4{0.f, 0.f, 0.f, 0.f};
    s1 = s0;
#pragma unroll
    for (int s = 0; s < 4; ++s) {
      s0 = __builtin_amdgcn_mfma_f32_16x16x32_bf16(asbf(kf[2 * s]), asbf(Qf[s]), s0, 0, 0, 0);
      s1 = __builtin_amdgcn_mfma_f32_16x16x32_bf16(asbf(kf[2 * s + 1]), asbf(Qf[s]), s1, 0, 0, 0);
    }
  };
  auto probs = [&](f32x4 s0, f32x4 s1) {
    const float mc = -m * CEXP;
    float p[8];
#pragma unroll
    for (int r = 0; r < 4; ++r) {
      p[r] = __builtin_amdgcn_exp2f(fmaf(s0[r], CEXP, mc));
      p[4 + r] = __builtin_amdgcn_exp2f(fmaf(s1[r], CEXP, mc));
    }
    l += ((p[0] + p[1]) + (p[2] + p[3])) + ((p[4] + p[5]) + (p[6] + p[7]));
    return u32x4{cvtpk(p[0], p[1]), cvtpk(p[2], p[3]), cvtpk(p[4], p[5]), cvtpk(p[6], p[7])};
  };
  auto rescale = [&](float mn) {
    const float alpha = __builtin_amdgcn_exp2f((m - mn) * CEXP);
    l *= alpha;
#pragma unroll
    for (int e = 0; e < 4; ++e) acc[e] *= alpha;
    m = mn;
  };
  auto values = [&](u32x4 P) {
    s16x4 vf[8];
#pragma unroll
    for (int cb = 0; cb < 4; ++cb) {
      const unsigned a = va0 ^ (cb << 5);
      vf[2 * cb] = __builtin_amdgcn_ds_read_tr16_b64_v4i16((LAS s16x4*)(ringp + KSLOT + a));
      vf[2 * cb + 1] = __builtin_amdgcn_ds_read_tr16_b64_v4i16((LAS s16x4*)(ringp + KSLOT + 2048 + a));
    }
    wait_lgkm();
    __builtin_amdgcn_sched_barrier(0);
#pragma unroll
    for (int cb = 0; cb < 4; ++cb) {
      const s16x8 f = __builtin_shufflevector(vf[2 * cb], vf[2 * cb + 1], 0, 1, 2, 3, 4, 5, 6, 7);
      acc[cb] = __builtin_amdgcn_mfma_f32_16x16x32_bf16(__builtin_bit_cast(bf16x8, f), asbf(P), acc[cb], 0, 0, 0);
    }
  };

#pragma unroll 1
  for (int r = 1; r < NR; ++r) {
    wait_vm<4>();
    __builtin_amdgcn_sched_barrier(0);
    f32x4 s0, s1;
    scores(s0, s1);
    const float mn = fmaxf(m, xmax(fmaxf(max4(s0), max4(s1))));
    if (__builtin_amdgcn_ballot_w64(mn > m)) rescale(mn);
    const u32x4 P = probs(s0, s1);
    cur = tile_off(r);
    const unsigned flip = ((r ^ (r - 1)) & 5) * 0x10000;
#pragma unroll
    for (int j = 0; j < 4; ++j) kv[j] ^= flip;
    vv[0] ^= flip;
    vv[1] ^= flip;
    __builtin_amdgcn_sched_barrier(0);
    { const unsigned t = now(); wait_vm<AHEAD_V>(); pace(2 * r, t); }
    issueK(cur);
    wait_vm<8>();
    __builtin_amdgcn_sched_barrier(0);
    values(P);
    { const unsigned t = now(); wait_vm<AHEAD_K>(); pace(2 * r + 1, t); }
    issueV(cur);
  }

  LAS char* const sm = (LAS char*)smem;
  wait_vm<4>();
  __builtin_amdgcn_sched_barrier(0);
  f32x4 s0, s1;
  scores(s0, s1);
  const float mn = fmaxf(m, xmax(fmaxf(max4(s0), max4(s1))));
  if (g == 0) *(LAS float*)(sm + LDS_SM + i * 32 + w * 4) = mn;
  wait_lgkm();
  __builtin_amdgcn_s_barrier();
  asm volatile("" ::: "memory");
  const f32x4 mw0 = *(LAS f32x4*)(sm + LDS_SM + i * 32), mw1 = *(LAS f32x4*)(sm + LDS_SM + i * 32 + 16);
  rescale(fmaxf(max4(mw0), max4(mw1)));
#pragma unroll
  for (int e = 0; e < 4; ++e) asm volatile("" : "+v"(acc[e]));
  const u32x4 P = probs(s0, s1);
  l = xsum(l);
  if (g == 0) *(LAS float*)(sm + LDS_SL + i * 32 + w * 4) = l;
  const int qq = (tid >> 4) & 15, jj = tid & 15;
  GAS char* dst = (GAS char*)out + (size_t)(bh * 16 + qq) * (D * 2) + half * 128 + jj * 8;
  unsigned wr = ring + i * OROW + g * 16, rd = lds0 + qq * OROW + jj * 16, rl = lds0 + LDS_SL + qq * 32;
  asm volatile("" : "+v"(dst), "+v"(wr), "+v"(rd), "+v"(rl));
  __builtin_amdgcn_sched_barrier(0);
  wait_vm<0>();
  __builtin_amdgcn_sched_barrier(0);
  values(P);
  {
    LAS char* d = (LAS char*)(size_t)wr;
#pragma unroll
    for (int cb = 0; cb < 4; ++cb) *(LAS f32x4*)(d + cb * 64) = acc[cb];
  }
  wait_lgkm();
  __builtin_amdgcn_s_barrier();
  asm volatile("" ::: "memory");

  if (w < 4) {
    const f32x4 lw0 = *(LAS f32x4*)(size_t)rl, lw1 = *(LAS f32x4*)(size_t)(rl + 16);
    f32x4 pa[NW];
#pragma unroll
    for (int ww = 0; ww < NW; ++ww) pa[ww] = *(LAS f32x4*)((LAS char*)(size_t)rd + ww * RING);
    const float inv = __builtin_amdgcn_rcpf(sum4(lw0) + sum4(lw1));
    const f32x4 o = (((pa[0] + pa[1]) + (pa[2] + pa[3])) + ((pa[4] + pa[5]) + (pa[6] + pa[7]))) * inv;
    *(GAS u32x2*)dst = u32x2{cvtpk(o[0], o[1]), cvtpk(o[2], o[3])};
  }
}

extern "C" void kernel_launch(void* const* d_in, const int* in_sizes, int n_in, void* d_out, int out_size, void* d_ws,
                              size_t ws_size, hipStream_t stream) {
  (void)hipFuncSetAttribute((const void*)attn_fwd, hipFuncAttributeMaxDynamicSharedMemorySize, LDS_BYTES);
  attn_fwd<<<NBH * 2, NTHREADS, LDS_BYTES, stream>>>(d_in[0], d_in[1], d_in[2], d_out);
}
```
